```python
import torch

def _softmax(x, axis=-1):
    return torch.softmax(x, dim=axis)
def _gelu(x):
    return torch.nn.functional.gelu(x, approximate="tanh")
def _silu(x):
    return torch.nn.functional.silu(x)
def _sigmoid(x):
    return torch.sigmoid(x)
def _layernorm(x, axis=-1, eps=1e-5):
    m = x.mean(dim=axis, keepdim=True)
    v = x.var(dim=axis, keepdim=True, unbiased=False)
    return (x - m) / torch.sqrt(v + eps)
def _scatter_add(tgt, idx, src, axis=0):
    return tgt.clone().index_add_(axis, idx, src)
def _segment_sum(src, seg, n):
    out = torch.zeros((n,) + src.shape[1:], dtype=src.dtype)
    return out.index_add_(0, seg, src)
def _pad(x, pw):
    flat = tuple(v for pair in reversed(pw) for v in pair)
    return torch.nn.functional.pad(x, flat)

kernel_name = 'parallel_sum__sliding_block_cached__dense_activation_mish__decode'

def setup_inputs():

    _w = (lambda a: (lambda t: t.to(torch.bfloat16) if t.is_floating_point() else t)(torch.as_tensor(a)))
    def _A_setup():
        B, Sq, D = 4, 1, 448
        Hq, Hkv, Sk, Dh = 16, 8, 4096, 256
        assert Hq % Hkv == 0, (Hq, Hkv)
        return {
            "x": _w((lambda *s: torch.randn(*s, dtype=torch.bfloat16) * 0.05)(B, Sq, D)),
            "k_cache": _w((lambda *s: torch.randn(*s, dtype=torch.bfloat16) * 1.6)(B, Hkv, Sk, Dh)),
            "v_cache": _w((lambda *s: torch.randn(*s, dtype=torch.bfloat16) * 0.4)(B, Hkv, Sk, Dh)),
            "wq": _w((lambda *s: torch.randn(*s, dtype=torch.bfloat16) * 1.6)(D, Hq * Dh) * (D ** -0.5)),
            "wo": _w((lambda *s: torch.randn(*s, dtype=torch.bfloat16) * 0.4)(Hq * Dh, D) * ((Hq * Dh) ** -0.5)),
        }
    _A_in = _A_setup()

    def _B_setup():
        B, S, D, F = 4, 1, 448, 1792
        return {
            "x": _w((lambda *s: torch.randn(*s, dtype=torch.bfloat16) * 0.05)(B, S, D)),
            "w1": _w((lambda *s: torch.randn(*s, dtype=torch.bfloat16) * 0.8)(D, F) * (D ** -0.5)),
            "b1": _w((lambda *s: torch.randn(*s, dtype=torch.bfloat16) * 0.2)(F)),
            "w2": _w((lambda *s: torch.randn(*s, dtype=torch.bfloat16) * 0.1)(F, D) * (F ** -0.5)),
        }
    _B_in = _B_setup()

    return {
        "A_x": _A_in["x"],
        "A_k_cache": _A_in["k_cache"],
        "A_v_cache": _A_in["v_cache"],
        "A_wq": _A_in["wq"],
        "A_wo": _A_in["wo"],
        "B_x": _B_in["x"],
        "B_w1": _B_in["w1"],
        "B_b1": _B_in["b1"],
        "B_w2": _B_in["w2"],
    }

def reference(A_x, A_k_cache, A_v_cache, A_wq, A_wo, B_x, B_w1, B_b1, B_w2):
    def _A_ref(x, k_cache, v_cache, wq, wo):
        B, Sq, D = x.shape
        Hq, Dh = 16, 256
        Hkv, Sk = k_cache.shape[1], k_cache.shape[2]
        W = min(4096, max(256, Sk // 4))
        rep = Hq // Hkv
        q = (lambda x, *d: x.permute(*d))((x @ wq).reshape(B, Sq, Hq, Dh), 0, 2, 1, 3)
        k = (lambda x, n, axis: torch.repeat_interleave(x, n, dim=axis))(k_cache[:, :, Sk - W:, :], rep, axis=1)
        v = (lambda x, n, axis: torch.repeat_interleave(x, n, dim=axis))(v_cache[:, :, Sk - W:, :], rep, axis=1)
        scores = torch.einsum("bhqd,bhkd->bhqk", q, k) * (Dh ** -0.5)
        p = _softmax(scores, axis=-1)
        o = torch.einsum("bhqk,bhkd->bhqd", p, v)
        return (lambda x, *d: x.permute(*d))(o, 0, 2, 1, 3).reshape(B, Sq, Hq * Dh) @ wo

    def _B_ref(x, w1, b1, w2):
        h = torch.einsum("bsd,df->bsf", x, w1) + b1
        sp = torch.log1p(torch.exp(-torch.abs(h))) + torch.where(h > 0, h, torch.zeros_like(h))
        act = h * torch.tanh(sp)
        return torch.einsum("bsf,fd->bsd", act, w2)

    x = A_x
    xn = _layernorm(x, axis=-1)
    return x + _A_ref(xn, A_k_cache, A_v_cache, A_wq, A_wo) + _B_ref(xn, B_w1, B_b1, B_w2)
```

```cpp
#include <hip/hip_runtime.h>
#include <hip/hip_bf16.h>
#include <stdint.h>

typedef unsigned short u16;
__device__ __forceinline__ float bf2f(u16 v) { return __uint_as_float(((unsigned)v) << 16); }
__device__ __forceinline__ u16 f2bf(float f) { __hip_bfloat16 h = __float2bfloat16(f); return *reinterpret_cast<u16*>(&h); }

constexpr int B = 4, D = 448, HQ = 16, HKV = 8, SK = 4096, DH = 256, W = 1024, F = 1792;
constexpr int NSPLIT = 8;

__device__ void layernorm_lds(const u16* x, float* xn  ) {
    int wave = threadIdx.x >> 6, lane = threadIdx.x & 63;
    float v[7]; float s = 0.f;
    for (int i = 0; i < 7; ++i) { v[i] = bf2f(x[wave * D + lane + 64 * i]); s += v[i]; }
    for (int m = 32; m >= 1; m >>= 1) s += __shfl_xor(s, m);
    float mean = s / D; float q = 0.f;
    for (int i = 0; i < 7; ++i) { float d = v[i] - mean; q += d * d; }
    for (int m = 32; m >= 1; m >>= 1) q += __shfl_xor(q, m);
    float var = q / D; float r = 1.0f / sqrtf(var + 1e-5f);
    for (int i = 0; i < 7; ++i) xn[wave * D + lane + 64 * i] = (v[i] - mean) * r;
    __syncthreads();
}

__global__ void __launch_bounds__(256) k1(const u16* x, const u16* wq, const u16* w1, const u16* b1, float* q, float* act) {
    __shared__ float xn[4 * D];
    layernorm_lds(x, xn);
    int col = blockIdx.x * 256 + threadIdx.x;
    if (col < HQ * DH) {
        float a[4] = {0, 0, 0, 0};
        for (int d = 0; d < D; ++d) { float w = bf2f(wq[(size_t)d * (HQ * DH) + col]); for (int b = 0; b < 4; ++b) a[b] += xn[b * D + d] * w; }
        for (int b = 0; b < 4; ++b) q[b * HQ * DH + col] = a[b];
    } else {
        int c = col - HQ * DH;
        if (c < F) {
            float a[4] = {0, 0, 0, 0};
            for (int d = 0; d < D; ++d) { float w = bf2f(w1[(size_t)d * F + c]); for (int b = 0; b < 4; ++b) a[b] += xn[b * D + d] * w; }
            for (int b = 0; b < 4; ++b) { float h = a[b] + bf2f(b1[c]); float sp = log1pf(expf(-fabsf(h))) + fmaxf(h, 0.f); act[b * F + c] = h * tanhf(sp); }
        }
    }
}

__global__ void __launch_bounds__(256) k2(const u16* kc, const u16* vc, const float* q, float* po, float* pml) {
    int s = blockIdx.x, kvh = blockIdx.y, b = blockIdx.z;
    __shared__ float qs[2][DH]; __shared__ float sc[2][128]; __shared__ float red[8];
    int tid = threadIdx.x;
    for (int i = tid; i < 512; i += 256) qs[i >> 8][i & 255] = q[b * HQ * DH + kvh * 512 + i];
    __syncthreads();
    const u16* kb = kc + ((size_t)(b * HKV + kvh) * SK + (SK - W) + s * 128) * DH;
    const u16* vb = vc + ((size_t)(b * HKV + kvh) * SK + (SK - W) + s * 128) * DH;
    int wave = tid >> 6, lane = tid & 63;
    for (int k = wave; k < 128; k += 4) {
        float a0 = 0, a1 = 0;
        for (int i = 0; i < 4; ++i) { float kv = bf2f(kb[(size_t)k * DH + lane + 64 * i]); a0 += kv * qs[0][lane + 64 * i]; a1 += kv * qs[1][lane + 64 * i]; }
        for (int m = 32; m >= 1; m >>= 1) { a0 += __shfl_xor(a0, m); a1 += __shfl_xor(a1, m); }
        if (lane == 0) { sc[0][k] = a0 * 0.0625f; sc[1][k] = a1 * 0.0625f; }
    }
    __syncthreads();
    float m0 = -INFINITY, m1 = -INFINITY;
    for (int k = 0; k < 128; ++k) { m0 = fmaxf(m0, sc[0][k]); m1 = fmaxf(m1, sc[1][k]); }
    __syncthreads();
    if (tid < 128) { sc[0][tid] = expf(sc[0][tid] - m0); sc[1][tid] = expf(sc[1][tid] - m1); }
    __syncthreads();
    float l0 = 0, l1 = 0, o0 = 0, o1 = 0;
    for (int k = 0; k < 128; ++k) { float v = bf2f(vb[(size_t)k * DH + tid]); l0 += sc[0][k]; l1 += sc[1][k]; o0 += sc[0][k] * v; o1 += sc[1][k] * v; }
    size_t r0 = ((size_t)(b * HQ + kvh * 2) * NSPLIT + s), r1 = ((size_t)(b * HQ + kvh * 2 + 1) * NSPLIT + s);
    po[r0 * DH + tid] = o0; po[r1 * DH + tid] = o1;
    if (tid == 0) { pml[r0 * 2] = m0; pml[r0 * 2 + 1] = l0; pml[r1 * 2] = m1; pml[r1 * 2 + 1] = l1; }
    (void)red;
}

__global__ void __launch_bounds__(256) k3(const float* po, const float* pml, float* o) {
    int bh = blockIdx.x; int tid = threadIdx.x;
    float M = -INFINITY; for (int s = 0; s < NSPLIT; ++s) M = fmaxf(M, pml[(bh * NSPLIT + s) * 2]);
    float L = 0, a = 0;
    for (int s = 0; s < NSPLIT; ++s) { float w = expf(pml[(bh * NSPLIT + s) * 2] - M); L += w * pml[(bh * NSPLIT + s) * 2 + 1]; a += w * po[((size_t)bh * NSPLIT + s) * DH + tid]; }
    o[(size_t)bh * DH + tid] = a / L;
}

__global__ void __launch_bounds__(256) k4(const u16* x, const float* o, const float* act, const u16* wo, const u16* w2, u16* out) {
    int b = blockIdx.y, c = blockIdx.x * 64 + (threadIdx.x & 63), wave = threadIdx.x >> 6;
    __shared__ float red[4][64];
    float a = 0;
    for (int r = wave; r < HQ * DH; r += 4) a += o[b * HQ * DH + r] * bf2f(wo[(size_t)r * D + c]);
    for (int r = wave; r < F; r += 4) a += act[b * F + r] * bf2f(w2[(size_t)r * D + c]);
    red[wave][threadIdx.x & 63] = a; __syncthreads();
    if (wave == 0) { float t = red[0][threadIdx.x] + red[1][threadIdx.x] + red[2][threadIdx.x] + red[3][threadIdx.x]; out[b * D + c] = f2bf(bf2f(x[b * D + c]) + t); }
}

extern "C" void kernel_launch(void* const* d_in, const int* in_sizes, int n_in, void* d_out, int out_size, void* d_ws, size_t ws_size, hipStream_t stream) {
    const u16* x = (const u16*)d_in[0]; const u16* kc = (const u16*)d_in[1]; const u16* vc = (const u16*)d_in[2];
    const u16* wq = (const u16*)d_in[3]; const u16* wo = (const u16*)d_in[4];
    const u16* w1 = (const u16*)d_in[6]; const u16* b1 = (const u16*)d_in[7]; const u16* w2 = (const u16*)d_in[8];
    float* ws = (float*)d_ws;
    float* q = ws; float* act = q + 4 * 4096; float* po = act + 4 * 2048; float* pml = po + 64 * 8 * 256; float* o = pml + 64 * 8 * 2;
    k1<<<(4096 + 1792) / 256, 256, 0, stream>>>(x, wq, w1, b1, q, act);
    k2<<<dim3(NSPLIT, HKV, B), 256, 0, stream>>>(kc, vc, q, po, pml);
    k3<<<64, 256, 0, stream>>>(po, pml, o);
    k4<<<dim3(7, 4), 256, 0, stream>>>(x, o, act, wo, w2, (u16*)d_out);
}
```

```cpp
#include <hip/hip_runtime.h>
#include <hip/hip_bf16.h>
#include <stdint.h>

#define GAS __attribute__((address_space(1)))
#define LAS __attribute__((address_space(3)))
typedef unsigned v4u __attribute__((ext_vector_type(4)));
typedef unsigned v2u __attribute__((ext_vector_type(2)));
typedef float f32x4 __attribute__((ext_vector_type(4)));
typedef short s16x4 __attribute__((ext_vector_type(4)));
typedef __bf16 bf16x8_t __attribute__((ext_vector_type(8)));

#define STAMPS 0
#define AMP_J 7
#define AMP_BLK 252
#define SETPRIO 3
#define QPOLL 10
#define PPOLL 10
#define WINL 8
#define PREFETCH_CODE 1

namespace fk {
constexpr int AUX_SC1 = 16;
constexpr int L_V = 0;
constexpr int L_X = 52736;
constexpr int L_W1 = 57344;
constexpr int L_R = 65536;
constexpr int L_WQ = L_R + 67584;
constexpr int L_M = L_R + 89088;
constexpr int L_Q = L_M;
constexpr int L_P = L_Q + 1024;
constexpr int L_AO = L_P + 4096;
constexpr int L_AX = L_AO + 512;
constexpr int L_F = L_AX + 512;
constexpr int F_S1 = L_F + 16, F_QK = L_F + 20, F_ACT = L_F + 24, F_AO = L_F + 28, F_Q = L_F + 32;
constexpr int LDS_BYTES = L_F + 64;
static_assert(L_WQ + 16384 <= L_M, "wq block inside region R");
static_assert(LDS_BYTES <= 163840, "LDS");
constexpr int WS_E = 0;
constexpr int WS_Q = 4096;
constexpr int WS_H = WS_Q + 4 * 8 * 4 * 128 * 16;
constexpr int WS_P = WS_H + 4 * 448 * 8 * 16;
constexpr int WS_S = WS_P + 32 * 32 * 16 * 16;
constexpr int WS_FIN = WS_S + 32 * 64 * 16;
constexpr int WS_ST = WS_FIN + 4 * 56 * 128 * 16;
constexpr int WS_END = WS_ST + 256 * 8 * 16 * 8;
constexpr unsigned SPIN_CAP = 1u << 17;

struct Args {
    const unsigned char* x; const unsigned char* k; const unsigned char* v; const unsigned char* wq; const unsigned char* wo;
    const unsigned char* w1; const unsigned char* b1; const unsigned char* w2; unsigned short* out; unsigned char* ws;
};

__device__ __forceinline__ unsigned long long rt() { return __builtin_amdgcn_s_memrealtime(); }
template <bool NT> __device__ __forceinline__ void dma16(unsigned voff, unsigned long long sbase, unsigned lds) {
    if (NT) asm volatile("s_mov_b32 m0, %2\n\ts_nop 0\n\tglobal_load_lds_dwordx4 %0, %1 nt" :: "v"(voff), "s"(sbase), "s"(lds) : "memory");
    else    asm volatile("s_mov_b32 m0, %2\n\ts_nop 0\n\tglobal_load_lds_dwordx4 %0, %1" :: "v"(voff), "s"(sbase), "s"(lds) : "memory");
}
template <int N> __device__ __forceinline__ void waitv() { asm volatile("s_waitcnt vmcnt(%0)" :: "i"(N) : "memory"); }
__device__ __forceinline__ void cbar() { asm volatile("" ::: "memory"); }

__device__ __forceinline__ f32x4 mfma(v4u a, v4u b, f32x4 c) {
    return __builtin_amdgcn_mfma_f32_16x16x32_bf16(__builtin_bit_cast(bf16x8_t, a), __builtin_bit_cast(bf16x8_t, b), c, 0, 0, 0);
}
__device__ __forceinline__ v4u lds128(unsigned a) { return *(const LAS v4u*)a; }
__device__ __forceinline__ v2u tr64(unsigned a) { return __builtin_bit_cast(v2u, __builtin_amdgcn_ds_read_tr16_b64_v4i16((LAS s16x4*)a)); }
__device__ __forceinline__ v4u trfrag(unsigned a0, unsigned a1) { v2u lo = tr64(a0), hi = tr64(a1); return v4u{lo.x, lo.y, hi.x, hi.y}; }
typedef __bf16 bf16x2_t __attribute__((ext_vector_type(2)));
typedef float f32x2 __attribute__((ext_vector_type(2)));
__device__ __forceinline__ unsigned cvtpk(float lo, float hi) { return __builtin_bit_cast(unsigned, __builtin_convertvector(f32x2{lo, hi}, bf16x2_t)); }
__device__ __forceinline__ float bflo(unsigned u) { return __builtin_bit_cast(float, u << 16); }
__device__ __forceinline__ float bfhi(unsigned u) { return __builtin_bit_cast(float, u & 0xffff0000u); }
template <int CTRL> __device__ __forceinline__ float dpp(float x) {
    return __builtin_bit_cast(float, __builtin_amdgcn_mov_dpp(__builtin_bit_cast(int, x), CTRL, 0xf, 0xf, true));
}
constexpr int XOR1 = 0xB1, XOR2 = 0x4E, HMIR = 0x141, ROR4 = 0x124, ROR8 = 0x128, NBC4 = 0x154;
__device__ __forceinline__ float xrow_max(float x) {
    auto s = __builtin_amdgcn_permlane16_swap(__float_as_uint(x), __float_as_uint(x), false, false);
    x = fmaxf(__uint_as_float(s[0]), __uint_as_float(s[1]));
    auto t = __builtin_amdgcn_permlane32_swap(__float_as_uint(x), __float_as_uint(x), false, false);
    return fmaxf(__uint_as_float(t[0]), __uint_as_float(t[1]));
}
__device__ __forceinline__ float x32_sum(float x) {
    auto t = __builtin_amdgcn_permlane32_swap(__float_as_uint(x), __float_as_uint(x), false, false);
    return __uint_as_float(t[0]) + __uint_as_float(t[1]);
}
__device__ __forceinline__ float xrow_sum(float x) {
    auto s = __builtin_amdgcn_permlane16_swap(__float_as_uint(x), __float_as_uint(x), false, false);
    return x32_sum(__uint_as_float(s[0]) + __uint_as_float(s[1]));
}
__device__ __forceinline__ float par16_max(float x) { x = fmaxf(x, dpp<XOR2>(x)); x = fmaxf(x, dpp<ROR4>(x)); return fmaxf(x, dpp<ROR8>(x)); }
__device__ __forceinline__ float par16_sum(float x) { x += dpp<XOR2>(x); x += dpp<ROR4>(x); return x + dpp<ROR8>(x); }
__device__ __forceinline__ float bperm(int srclane, float v) { return __builtin_bit_cast(float, __builtin_amdgcn_ds_bpermute(srclane * 4, __builtin_bit_cast(int, v))); }

__device__ __forceinline__ void wait_lds_ge(unsigned addr, unsigned want) {
#pragma unroll 1
    for (unsigned i = 0; i < (1u << 22); ++i) { if ((unsigned)__builtin_amdgcn_readfirstlane(*(volatile LAS unsigned*)addr) >= want) break; __builtin_amdgcn_s_sleep(1); }
    cbar();
}
__device__ __forceinline__ unsigned prog_min() {
    const v4u p = *(volatile LAS v4u*)L_F;
    const unsigned m01 = p.x < p.y ? p.x : p.y, m23 = p.z < p.w ? p.z : p.w;
    return __builtin_amdgcn_readfirstlane(m01 < m23 ? m01 : m23);
}
__device__ __forceinline__ void wait_prog(unsigned want) {
#pragma unroll 1
    for (unsigned i = 0; i < (1u << 22); ++i) { if (prog_min() >= want) break; __builtin_amdgcn_s_sleep(1); }
    cbar();
}
__device__ __forceinline__ void lds_signal(unsigned addr) {
    asm volatile("s_waitcnt lgkmcnt(0)\n\ts_mov_b64 exec, 1\n\tds_add_u32 %0, %1\n\ts_mov_b64 exec, -1" :: "v"(addr), "v"(1u) : "memory");
}
__device__ __forceinline__ bool tags_ok(v4u g, unsigned t0, unsigned t1) { return (g.y == t0) & (g.w == t1); }
__device__ __forceinline__ v4u ldg(__amdgpu_buffer_rsrc_t rs, int off) { return __builtin_amdgcn_raw_buffer_load_b128(rs, off, 0, AUX_SC1); }
__device__ __forceinline__ void stg(__amdgpu_buffer_rsrc_t rs, int off, unsigned p0, unsigned p1, unsigned t0, unsigned t1) { __builtin_amdgcn_raw_buffer_store_b128(v4u{p0, t0, p1, t1}, rs, off, 0, AUX_SC1); }

#define STAMP(i) do { } while (0)
#define NPOLL(i) do { } while (0)
#define NPOLL2(i) do { } while (0)
#define STAMP_DECL do { } while (0)
#define STAMP_FLUSH() do { } while (0)

__global__ void __launch_bounds__(512, 2) fused(Args a) {
    const int tid = threadIdx.x, lane = tid & 63, w = __builtin_amdgcn_readfirstlane(tid >> 6);
    const int blk = blockIdx.x, kvh = blk & 7, j = blk >> 3, b = j >> 3, s = j & 7;
    const bool hrole = blk < 196;
    const int nq = b < 2 ? 4 : 3;
    const unsigned nW = 1 + nq + (hrole ? 2 : 0);
    STAMP_DECL;
    STAMP(0);
    if (__builtin_expect(w < 4, 1)) {
        const int L = w;
        if (lane < 56) dma16<false>(lane * 16, (unsigned long long)a.x + L * 896, L_X + L * 912);
        const unsigned f = ((lane >> 4) & 1) | ((L & 1) << 1), sp = lane & 7, gp = (((sp >> 1) ^ f) << 1) | (sp & 1);
        {
            const int r0 = b < 2 ? b * 128 : 256 + (b - 2) * 96;
            unsigned long long p = (unsigned long long)a.wq + (size_t)(r0 + 8 * L) * 8192 + (kvh * 512 + s * 64) * 2; unsigned l = L_WQ + L * 1024;
            const unsigned voff = (lane >> 3) * 8192 + gp * 16;
#pragma unroll 1
            for (int i = 0; i < nq; ++i) { dma16<true>(voff, p, l); p += 32 * 8192; l += 4096; }
        }
        if (hrole) {
            const int cb = blk % 28, rb = blk / 28;
            unsigned long long p = (unsigned long long)a.w1 + (size_t)(rb * 64 + 8 * L) * 3584 + cb * 128;
            const unsigned voff = (lane >> 3) * 3584 + gp * 16;
            dma16<true>(voff, p, L_W1 + L * 1024);
            dma16<true>(voff, p + 32 * 3584, L_W1 + L * 1024 + 4096);
        }
        const size_t kvbase = ((size_t)(b * 8 + kvh) * 4096 + 3072) * 512;
        unsigned n = nW;
        volatile LAS unsigned* prog = (volatile LAS unsigned*)(L_F + 4 * L);
        {
            const unsigned voff = (lane >> 5) * 512 + ((lane & 31) ^ (lane >> 5)) * 16;
            unsigned long long p = (unsigned long long)a.k + kvbase + (size_t)s * 65536 + L * 1024; unsigned l = L_R + L * 1056;
            dma16<true>(voff, p, l); ++n; p += 4096; l += 4224;
            __builtin_amdgcn_s_barrier();
#pragma unroll 1
            for (int i = 1; i < 16; ++i) { dma16<true>(voff, p, l); ++n; waitv<WINL - 1>(); const int d = (int)n - (WINL - 1); *prog = d > 0 ? d : 0; p += 4096; l += 4224; }
        }
        STAMP(1);
        {
            const unsigned voff = (lane >> 2) * 512 + ((lane & 3) ^ ((lane >> 5) << 1)) * 16;
            unsigned long long p = (unsigned long long)a.v + kvbase + s * 64 + (size_t)L * 8192; unsigned l = L_V + L * 1024;
#pragma unroll 1
            for (int i = 0; i < 16; ++i) {
                if (i == 12) wait_lds_ge(F_S1, 4);
                dma16<true>(voff, p, l); ++n; waitv<WINL - 1>(); *prog = n - (WINL - 1); p += 32768; l += 4096;
            }
        }
        STAMP(2);
        wait_lds_ge(F_QK, 4);
        STAMP(4);
        {
            const unsigned voff = lane * 16;
            const unsigned long long w2p = (unsigned long long)a.w2, wop = (unsigned long long)a.wo;
            const int f0 = (kvh * 8 + s) * 28;
            unsigned l = L_R + L * 928;
#pragma unroll 1
            for (int i = 0; i < 8; ++i) {
                int r = f0 + L + 4 * i; r = r < 1791 ? r : 1791;
                if (lane < 56) dma16<true>(voff, w2p + (unsigned)(r * 896), l);
                ++n; waitv<WINL - 1>(); *prog = n - (WINL - 1); l += 3712;
            }
            STAMP(5);
            unsigned long long p = wop + (unsigned)((kvh * 512 + s * 32 + L) * 896);
#pragma unroll 1
            for (int i = 0; i < 16; ++i) {
                if (lane < 56) dma16<true>(voff, p, l);
                p += (i == 7) ? (256 - 28) * 896 : 4 * 896; l += 3712;
            }
            n += 16;
            STAMP(6);
            waitv<16>(); *prog = n - 16;
            STAMP(7);
            waitv<8>(); *prog = n - 8;
            STAMP(8);
            waitv<0>(); *prog = n;
        }
        STAMP(3);
        STAMP_FLUSH();
        return;
    }
    const int t = w - 4, n = lane & 15, g = lane >> 4;
    __builtin_amdgcn_s_setprio(SETPRIO);
    if (t == 0) {
        if (lane < 16) *(LAS unsigned*)(L_F + 4 * lane) = 0u;
        if (lane < 56) *(LAS v4u*)(L_X + 4 * 912 + lane * 16) = v4u{0x3F803F80u, 0x3F803F80u, 0x3F803F80u, 0x3F803F80u};
    } else if (t == 1) {
        if (lane < 32) *(LAS v4u*)(L_AX + lane * 16) = v4u{0u, 0u, 0u, 0u};
    }
    asm volatile("s_waitcnt lgkmcnt(0)" ::: "memory");
    __builtin_amdgcn_s_barrier();
    cbar();
    {
        unsigned long long pc;
        asm volatile("s_getpc_b64 %0" : "=s"(pc));
        pc = (pc & ~0xFFull) + t * 3072;
        dma16<false>(lane * 16, pc, L_P + t * 1024);
        dma16<false>(lane * 16, pc + 1024, L_P + t * 1024);
        dma16<false>(lane * 16, pc + 2048, L_P + t * 1024);
    }
    const __amdgpu_buffer_rsrc_t rs = __builtin_amdgcn_make_buffer_rsrc(a.ws, 0, WS_END, 0x00020000);
    const unsigned Eu = *(const __attribute__((address_space(4))) unsigned*)(a.ws + WS_E);
    const int pidx = kvh * 8 + s, gid = b * 8 + kvh;
    v2u b1v = v2u{0u, 0u};
    if (t == 1 && lane < 56) b1v = *(const GAS v2u*)(a.b1 + (pidx * 28 + (lane >> 3) * 4) * 2);
    unsigned xres = 0; int ocol = 0;
    if (t == 3 && pidx < 56 && lane < 8) { ocol = 16 * (4 * (pidx >> 3) + (lane >> 1)) + 2 * (pidx & 7) + (lane & 1); xres = *(const GAS unsigned short*)(a.x + (b * 448 + ocol) * 2); }
    const unsigned r1 = n >> 2, c4 = n & 3;

    {
        const unsigned xa = L_X + (n < 4 ? n : 4) * 912 + g * 16;
        const unsigned fs = ((r1 >> 1) & 1) | ((g & 1) << 1);
        const unsigned wo_ = (8 * g + r1) * 128 + ((t ^ fs) * 32) + c4 * 8;
        const int ks0 = b < 2 ? b * 4 : 8 + (b - 2) * 3, rb = blk / 28;
        const int qoff = WS_Q + (((n * 8 + kvh) * 4 + b) * 128 + s * 16 + t * 4 + g) * 16;
        const int hoff = WS_H + ((n * 448 + (blk % 28) * 16 + t * 4 + g) * 8 + rb) * 16;
        wait_prog(1);
        v4u xf[14];
#pragma unroll
        for (int ks = 0; ks < 14; ++ks) xf[ks] = lds128(xa + 64 * ks);
        v4u bq[4], bh[2];
#pragma unroll
        for (int kk = 0; kk < 4; ++kk) bq[kk] = lds128(xa + 64 * (ks0 + (kk < nq ? kk : 0)));
#pragma unroll
        for (int kk = 0; kk < 2; ++kk) bh[kk] = lds128(xa + 64 * ((hrole ? 2 * rb : 0) + kk));
        __builtin_amdgcn_sched_barrier(0);
        f32x4 G0 = {0.f, 0.f, 0.f, 0.f}, G1 = {0.f, 0.f, 0.f, 0.f};
#pragma unroll
        for (int ks = 0; ks < 14; ks += 2) { G0 = mfma(xf[ks], xf[ks], G0); G1 = mfma(xf[ks + 1], xf[ks + 1], G1); }
        const f32x4 G = G0 + G1;
        const float gsel = n == 0 ? G[0] : n == 1 ? G[1] : n == 2 ? G[2] : G[3];
        const float sq = bperm(n & 3, gsel), sm = bperm(16 + (n & 3), G[0]);
        const float mean = sm * (1.0f / 448.0f);
        const float var = fmaxf(fmaf(sq, 1.0f / 448.0f, -mean * mean), 0.f);
        const float rstd = __builtin_amdgcn_rsqf(var + 1e-5f), mr = mean * rstd;
        const unsigned T0 = Eu * 0x9E3779B1u + 0x7F4A7C15u, T1 = (Eu ^ 0x5BD1E995u) * 0x85EBCA6Bu + 0x165667B1u;
        asm volatile("" :: "v"(rstd), "v"(mr), "v"(qoff), "v"(hoff));
        wait_prog(nW);
        STAMP(1);
        v4u aq[4], ah[2];
#pragma unroll
        for (int kk = 0; kk < 4; ++kk) aq[kk] = trfrag(L_WQ + wo_ + kk * 4096, L_WQ + wo_ + kk * 4096 + 512);
#pragma unroll
        for (int kk = 0; kk < 2; ++kk) ah[kk] = trfrag(L_W1 + wo_ + kk * 4096, L_W1 + wo_ + kk * 4096 + 512);
        __builtin_amdgcn_sched_barrier(0);
        f32x4 Dq = {0.f, 0.f, 0.f, 0.f}, Dq2 = {0.f, 0.f, 0.f, 0.f}, Dh = {0.f, 0.f, 0.f, 0.f};
        Dq = mfma(aq[0], bq[0], Dq);
        Dq2 = mfma(aq[1], bq[1], Dq2);
        Dh = mfma(ah[0], bh[0], Dh);
        Dq = mfma(aq[2], bq[2], Dq);
        if (nq == 4) Dq2 = mfma(aq[3], bq[3], Dq2);
        Dh = mfma(ah[1], bh[1], Dh);
        Dq += Dq2;
        lds_signal(F_S1);
        float q[4], h[4];
#pragma unroll
        for (int r = 0; r < 4; ++r) { q[r] = fmaf(rstd, Dq[r], -mr * dpp<NBC4>(Dq[r])); h[r] = fmaf(rstd, Dh[r], -mr * dpp<NBC4>(Dh[r])); }
        if (n < 4) {
            stg(rs, qoff, cvtpk(q[0], q[1]), cvtpk(q[2], q[3]), T0, T1);
            if (hrole) stg(rs, hoff, cvtpk(h[0], h[1]), cvtpk(h[2], h[3]), T0, T1);
        }
        STAMP(2);

        {
            const int qo = WS_Q + (gid * 512 + (lane >> 5) * 128 + 32 * t + (lane & 31)) * 16;
            v4u x0, x1;
            wait_prog(nW + QPOLL);
#pragma unroll 1
            for (unsigned it = 0; it < SPIN_CAP; ++it) {
                cbar();
                NPOLL(9);
                x0 = ldg(rs, qo); x1 = ldg(rs, qo + 256 * 16);
                if (__all(tags_ok(x0, T0, T1) & tags_ok(x1, T0, T1))) break;
            }
            STAMP(3);
            const float q0 = x32_sum(bflo(x0.x) + bflo(x1.x)), q1 = x32_sum(bfhi(x0.x) + bfhi(x1.x));
            const float q2 = x32_sum(bflo(x0.z) + bflo(x1.z)), q3 = x32_sum(bfhi(x0.z) + bfhi(x1.z));
            if (lane < 32) *(LAS v2u*)(L_Q + (32 * t + lane) * 8) = v2u{cvtpk(q0, q1), cvtpk(q2, q3)};
            lds_signal(F_Q);
        }
        if (t == 1) {
            const bool val = lane < 56 && (lane & 7) < 7;
            const int ho = WS_H + (b * 448 + pidx * 7) * 128 + (lane < 56 ? lane : 0) * 16;
            v4u x;
#pragma unroll 1
            for (unsigned it = 0; it < SPIN_CAP; ++it) {
                cbar();
                x = ldg(rs, ho);
                if (__all(!val || tags_ok(x, T0, T1))) break;
            }
            const float hv[4] = {val ? bflo(x.x) : 0.f, val ? bfhi(x.x) : 0.f, val ? bflo(x.z) : 0.f, val ? bfhi(x.z) : 0.f};
            const float bb[4] = {bflo(b1v.x), bfhi(b1v.x), bflo(b1v.y), bfhi(b1v.y)};
            float act[4];
#pragma unroll
            for (int r = 0; r < 4; ++r) {
                float v = hv[r]; v += dpp<XOR1>(v); v += dpp<XOR2>(v); v += dpp<HMIR>(v); v += bb[r];
                const float e = __builtin_amdgcn_exp2f(-1.4426950408889634f * fabsf(v));
                const float num = v > 0.f ? fmaf(2.f, e, 1.f) : e * (e + 2.f);
                const float den = v > 0.f ? fmaf(2.f * e, e, num) : num + 2.f;
                act[r] = v * num * __builtin_amdgcn_rcpf(den);
            }
            const int uq = lane >> 3;
            if (lane < 56 && (lane & 7) == 0) *(LAS v2u*)(L_AX + (8 * (uq & 3) + 4 * (uq >> 2)) * 2) = v2u{cvtpk(act[0], act[1]), cvtpk(act[2], act[3])};
            lds_signal(F_ACT);
        }
        {
            const unsigned ka = L_R + t * 16896 + (n >> 1) * 1056 + (n & 1) * 512 + ((g ^ (n & 1)) * 16);
            const int sub = 4 * s + t;
            const bool isp = n < 4, iss = (n >> 1) == 2 && g == 0;
            const int poff = isp ? WS_P + ((gid * 32 + sub) * 16 + g * 4 + n) * 16 : WS_S + ((gid * 32 + sub) * 2 + (n & 1)) * 16;
            wait_prog(nW + 4 * (t + 1));
            v4u kf[16];
#pragma unroll
            for (int i = 0; i < 16; ++i) kf[i] = lds128(ka + (i >> 3) * 8448 + (i & 7) * 64);
            wait_lds_ge(F_Q, 4);
            v4u qf[8];
#pragma unroll
            for (int ks = 0; ks < 8; ++ks) qf[ks] = lds128(L_Q + (n & 1) * 512 + ks * 64 + g * 16);
            __builtin_amdgcn_sched_barrier(0);
            f32x4 z0 = {0.f, 0.f, 0.f, 0.f}, z1 = {0.f, 0.f, 0.f, 0.f};
#pragma unroll
            for (int ks = 0; ks < 8; ++ks) { z0 = mfma(kf[ks], qf[ks], z0); z1 = mfma(kf[8 + ks], qf[ks], z1); }
            lds_signal(F_QK);
            constexpr float C = 0.0625f * 1.4426950408889634f;
            const float z[4] = {(n & 2) ? z1[0] : z0[0], (n & 2) ? z1[1] : z0[1], (n & 2) ? z1[2] : z0[2], (n & 2) ? z1[3] : z0[3]};
            float m = fmaxf(fmaxf(z[0], z[1]), fmaxf(z[2], z[3])) * C;
            m = xrow_max(fmaxf(m, dpp<XOR2>(m)));
            float p[4];
#pragma unroll
            for (int r = 0; r < 4; ++r) p[r] = __builtin_amdgcn_exp2f(fmaf(z[r], C, -m));
            float l = (p[0] + p[1]) + (p[2] + p[3]);
            l = xrow_sum(l + dpp<XOR2>(l));
            if (isp | iss) stg(rs, poff, isp ? cvtpk(p[0], p[1]) : __float_as_uint(m), isp ? cvtpk(p[2], p[3]) : __float_as_uint(l), T0, T1);
        }
        STAMP(4);
        {
            v4u pg0, pg1, st;
            const int po = WS_P + ((gid * 32 + 8 * t) * 16 + lane) * 16, so = WS_S + (gid * 64 + lane) * 16;
            wait_prog(nW + 16 + PPOLL);
#pragma unroll 1
            for (unsigned it = 0; it < SPIN_CAP; ++it) {
                cbar();
                NPOLL2(9);
                pg0 = ldg(rs, po); pg1 = ldg(rs, po + 1024); st = ldg(rs, so);
                if (__all(tags_ok(pg0, T0, T1) & tags_ok(pg1, T0, T1) & tags_ok(st, T0, T1))) break;
            }
            STAMP(5);
            const float m = __uint_as_float(st.x), l = __uint_as_float(st.z);
            const float M = xrow_max(par16_max(m)), e = __builtin_amdgcn_exp2f(m - M);
            const float sc = e * __builtin_amdgcn_rcpf(xrow_sum(par16_sum(l * e)));
            const int sl = 16 * t + 2 * g + (lane & 1);
            const float s0 = bperm(sl, sc), s1 = bperm(sl + 8, sc);
            const unsigned pw = L_P + t * 1024 + (lane & 1) * 512 + (32 * g + 16 * ((lane >> 1) & 1) + 4 * ((lane >> 2) & 3)) * 2;
            const v2u w0_ = v2u{cvtpk(bflo(pg0.x) * s0, bfhi(pg0.x) * s0), cvtpk(bflo(pg0.z) * s0, bfhi(pg0.z) * s0)};
            const v2u w1_ = v2u{cvtpk(bflo(pg1.x) * s1, bfhi(pg1.x) * s1), cvtpk(bflo(pg1.z) * s1, bfhi(pg1.z) * s1)};
            *(LAS v2u*)(pw) = w0_;
            *(LAS v2u*)(pw + 256) = w1_;
            cbar();
            const unsigned vrow = L_V + t * 16384 + (8 * g + r1) * 64 + c4 * 8;
            const unsigned va0 = vrow + (g & 1) * 32, va1 = vrow + ((g & 1) ^ 1) * 32;
            const unsigned pr = L_P + t * 1024 + (n & 1) * 512 + g * 16;
            v4u pf[8];
#pragma unroll
            for (int kk = 0; kk < 8; ++kk) pf[kk] = lds128(pr + kk * 64);
            wait_prog(nW + 16 + 4 * (t + 1));
            v4u vf0[8], vf1[8];
#pragma unroll
            for (int kk = 0; kk < 8; ++kk) { vf0[kk] = trfrag(va0 + kk * 2048, va0 + kk * 2048 + 256); vf1[kk] = trfrag(va1 + kk * 2048, va1 + kk * 2048 + 256); }
            __builtin_amdgcn_sched_barrier(0);
            f32x4 o0 = {0.f, 0.f, 0.f, 0.f}, o1 = {0.f, 0.f, 0.f, 0.f};
#pragma unroll
            for (int kk = 0; kk < 8; ++kk) { o0 = mfma(vf0[kk], pf[kk], o0); o1 = mfma(vf1[kk], pf[kk], o1); }
            if (n < 2) *(LAS v4u*)(L_AO + t * 128 + n * 64 + g * 16) = v4u{cvtpk(o0[0], o0[1]), cvtpk(o0[2], o0[3]), cvtpk(o1[0], o1[1]), cvtpk(o1[2], o1[3])};
            lds_signal(F_AO);
        }
        STAMP(6);
        {
            const unsigned ba = L_R + (4 * g + r1) * 928 + t * 256 + c4 * 8;
            const int u = 2 * t + g;
            const int foff = WS_FIN + (((b * 56 + 8 * u + (n >> 1)) * 64 + pidx) * 2 + (n & 1)) * 16;
            f32x4 acc[8];
            v4u bf[8];
            wait_lds_ge(F_ACT, 1);
            const v4u AX = lds128(L_AX + (n & 3) * 128 + g * 16);
            wait_prog(nW + 32 + 8);
#pragma unroll
            for (int T = 0; T < 8; ++T) bf[T] = trfrag(ba + T * 32, ba + T * 32 + 16 * 928);
            __builtin_amdgcn_sched_barrier(0);
#pragma unroll
            for (int T = 0; T < 8; ++T) acc[T] = mfma(AX, bf[T], f32x4{0.f, 0.f, 0.f, 0.f});
            wait_lds_ge(F_AO, 4);
            const v4u A1 = lds128(L_AO + (n & 3) * 128 + g * 16), A2 = lds128(L_AO + (n & 3) * 128 + 64 + g * 16);
            wait_prog(nW + 32 + 16);
#pragma unroll
            for (int T = 0; T < 8; ++T) bf[T] = trfrag(ba + 32 * 928 + T * 32, ba + 48 * 928 + T * 32);
            __builtin_amdgcn_sched_barrier(0);
#pragma unroll
            for (int T = 0; T < 8; ++T) acc[T] = mfma(A1, bf[T], acc[T]);
            wait_prog(nW + 32 + 24);
#pragma unroll
            for (int T = 0; T < 8; ++T) bf[T] = trfrag(ba + 64 * 928 + T * 32, ba + 80 * 928 + T * 32);
            __builtin_amdgcn_sched_barrier(0);
#pragma unroll
            for (int T = 0; T < 8; ++T) acc[T] = mfma(A2, bf[T], acc[T]);
            float v[4];
#pragma unroll
            for (int r = 0; r < 4; ++r) {
                const float lo = (acc[r][0] + acc[r][1]) + (acc[r][2] + acc[r][3]), hi = (acc[4 + r][0] + acc[4 + r][1]) + (acc[4 + r][2] + acc[4 + r][3]);
                v[r] = (g & 1) ? hi : lo;
            }
            if (g < (t < 3 ? 2 : 1)) stg(rs, foff, cvtpk(v[0], v[1]), cvtpk(v[2], v[3]), T0, T1);
        }
        STAMP(7);
        if (t == 3 && pidx < 56) {
            v4u f0, f1;
            const int fo = WS_FIN + ((b * 56 + pidx) * 128 + lane) * 16;
#pragma unroll 1
            for (unsigned it = 0; it < SPIN_CAP; ++it) {
                cbar();
                f0 = ldg(rs, fo); f1 = ldg(rs, fo + 1024);
                if (__all(tags_ok(f0, T0, T1) & tags_ok(f1, T0, T1))) break;
            }
            float v[4] = {bflo(f0.x) + bflo(f1.x), bfhi(f0.x) + bfhi(f1.x), bflo(f0.z) + bflo(f1.z), bfhi(f0.z) + bfhi(f1.z)};
#pragma unroll
            for (int r = 0; r < 4; ++r) v[r] = xrow_sum(par16_sum(v[r]));
            const int r = lane >> 1;
            const float val = r == 0 ? v[0] : r == 1 ? v[1] : r == 2 ? v[2] : v[3];
            if (lane < 8) {
                asm volatile("" : "+v"(xres));
                const float res = bflo(xres) + val;
                a.out[b * 448 + ocol] = (unsigned short)(cvtpk(res, res) & 0xffffu);
            }
            if (blk == 0 && lane == 0) *(GAS unsigned*)(a.ws + WS_E) = Eu + 1u;
            STAMP(8);
        }
        STAMP_FLUSH();
    }
}
}

extern "C" void kernel_launch(void* const* d_in, const int* in_sizes, int n_in, void* d_out, int out_size, void* d_ws, size_t ws_size, hipStream_t stream) {
    (void)in_sizes; (void)n_in; (void)out_size; (void)ws_size;
    fk::Args a;
    a.x = (const unsigned char*)d_in[0]; a.k = (const unsigned char*)d_in[1]; a.v = (const unsigned char*)d_in[2];
    a.wq = (const unsigned char*)d_in[3]; a.wo = (const unsigned char*)d_in[4]; a.w1 = (const unsigned char*)d_in[6];
    a.b1 = (const unsigned char*)d_in[7]; a.w2 = (const unsigned char*)d_in[8]; a.out = (unsigned short*)d_out; a.ws = (unsigned char*)d_ws;
    (void)hipFuncSetAttribute((const void*)fk::fused, hipFuncAttributeMaxDynamicSharedMemorySize, fk::LDS_BYTES);
    hipLaunchKernelGGL(fk::fused, dim3(256), dim3(512), fk::LDS_BYTES, stream, a);
}
```

```cpp
#include <hip/hip_runtime.h>
#include <hip/hip_bf16.h>
#include <stdint.h>

#define GAS __attribute__((address_space(1)))
#define LAS __attribute__((address_space(3)))
typedef unsigned v4u __attribute__((ext_vector_type(4)));
typedef unsigned v2u __attribute__((ext_vector_type(2)));
typedef float f32x4 __attribute__((ext_vector_type(4)));
typedef float f32x2 __attribute__((ext_vector_type(2)));
typedef short s16x4 __attribute__((ext_vector_type(4)));
typedef __bf16 bf16x8_t __attribute__((ext_vector_type(8)));
typedef __bf16 bf16x2_t __attribute__((ext_vector_type(2)));

#define STAMPS 0
#define SETPRIO 3
#define QPOLL 10
#define FPROBE 2
#define WINL 8
#define PREFETCH_CODE 1

namespace fk {
constexpr int AUX_SC1 = 16;
constexpr int L_K = 0;
constexpr int L_WQ = 67584;
constexpr int L_V = L_WQ + 16384;
constexpr int L_X = L_V + 69632 - 8192 - 4608;
constexpr int L_W1 = L_V + 69632 - 8192;
constexpr int L_M = L_V + 69632;
constexpr int L_Q = L_M;
constexpr int L_P = L_Q + 1024;
constexpr int L_A = L_P + 512;
constexpr int L_AM = L_A + 256;
constexpr int L_MX = L_AM + 128;
constexpr int L_LS = L_MX + 32;
constexpr int L_F = L_LS + 32;
constexpr int F_S1 = L_F + 16, F_QK = L_F + 20, F_M = L_F + 24, F_P = L_F + 28, F_Q = L_F + 32, F_A = L_F + 36, F_AM = L_F + 40;
constexpr int L_PF = L_F + 64;
constexpr int LDS_BYTES = L_PF + 1024;
static_assert(L_X >= L_V + 52 * 1088, "X / W1 images alias V instructions >= 52 only");
static_assert(48 * 928 <= 67584, "projection rows inside the K image");
static_assert(LDS_BYTES <= 163840, "LDS");
static_assert((L_F & 15) == 0 && (L_MX & 15) == 0 && (L_A & 15) == 0, "alignment");
constexpr int WS_E = 0;
constexpr int WS_Q = 4096;
constexpr int WS_H = WS_Q + 4 * 8 * 4 * 128 * 16;
constexpr int WS_O = WS_H + 4 * 448 * 8 * 16;
constexpr int WS_S = WS_O + 128 * 256 * 16;
constexpr int WS_FIN = WS_S + 512 * 16;
constexpr int WS_ST = WS_FIN + 4 * 56 * 256 * 16;
constexpr int WS_END = WS_ST + 256 * 8 * 16 * 8;
constexpr unsigned SPIN_CAP = 1u << 17;

struct Args {
    const unsigned char* x; const unsigned char* k; const unsigned char* v; const unsigned char* wq; const unsigned char* wo;
    const unsigned char* w1; const unsigned char* b1; const unsigned char* w2; unsigned short* out; unsigned char* ws;
};

__device__ __forceinline__ unsigned long long rt() { return __builtin_amdgcn_s_memrealtime(); }
template <bool NT> __device__ __forceinline__ void dma16(unsigned voff, unsigned long long sbase, unsigned lds) {
    if (NT) asm volatile("s_mov_b32 m0, %2\n\ts_nop 0\n\tglobal_load_lds_dwordx4 %0, %1 nt" :: "v"(voff), "s"(sbase), "s"(lds) : "memory");
    else    asm volatile("s_mov_b32 m0, %2\n\ts_nop 0\n\tglobal_load_lds_dwordx4 %0, %1" :: "v"(voff), "s"(sbase), "s"(lds) : "memory");
}
template <int N> __device__ __forceinline__ void waitv() { asm volatile("s_waitcnt vmcnt(%0)" :: "i"(N) : "memory"); }
__device__ __forceinline__ void cbar() { asm volatile("" ::: "memory"); }

__device__ __forceinline__ f32x4 mfma(v4u a, v4u b, f32x4 c) {
    return __builtin_amdgcn_mfma_f32_16x16x32_bf16(__builtin_bit_cast(bf16x8_t, a), __builtin_bit_cast(bf16x8_t, b), c, 0, 0, 0);
}
__device__ __forceinline__ v4u lds128(unsigned a) { return *(const LAS v4u*)a; }
__device__ __forceinline__ v2u lds64(unsigned a) { return *(const LAS v2u*)a; }
__device__ __forceinline__ v2u tr64(unsigned a) { return __builtin_bit_cast(v2u, __builtin_amdgcn_ds_read_tr16_b64_v4i16((LAS s16x4*)a)); }
__device__ __forceinline__ v4u trfrag(unsigned a0, unsigned a1) { v2u lo = tr64(a0), hi = tr64(a1); return v4u{lo.x, lo.y, hi.x, hi.y}; }
__device__ __forceinline__ unsigned cvtpk(float lo, float hi) { return __builtin_bit_cast(unsigned, __builtin_convertvector(f32x2{lo, hi}, bf16x2_t)); }
__device__ __forceinline__ float bflo(unsigned u) { return __builtin_bit_cast(float, u << 16); }
__device__ __forceinline__ float bfhi(unsigned u) { return __builtin_bit_cast(float, u & 0xffff0000u); }
template <int CTRL> __device__ __forceinline__ float dpp(float x) {
    return __builtin_bit_cast(float, __builtin_amdgcn_mov_dpp(__builtin_bit_cast(int, x), CTRL, 0xf, 0xf, true));
}
constexpr int XOR1 = 0xB1, XOR2 = 0x4E, HMIR = 0x141, ROR4 = 0x124, ROR8 = 0x128, NBC4 = 0x154;
__device__ __forceinline__ float xrow_max(float x) {
    auto s = __builtin_amdgcn_permlane16_swap(__float_as_uint(x), __float_as_uint(x), false, false);
    x = fmaxf(__uint_as_float(s[0]), __uint_as_float(s[1]));
    auto t = __builtin_amdgcn_permlane32_swap(__float_as_uint(x), __float_as_uint(x), false, false);
    return fmaxf(__uint_as_float(t[0]), __uint_as_float(t[1]));
}
__device__ __forceinline__ float x32_sum(float x) {
    auto t = __builtin_amdgcn_permlane32_swap(__float_as_uint(x), __float_as_uint(x), false, false);
    return __uint_as_float(t[0]) + __uint_as_float(t[1]);
}
__device__ __forceinline__ float xrow_sum(float x) {
    auto s = __builtin_amdgcn_permlane16_swap(__float_as_uint(x), __float_as_uint(x), false, false);
    return x32_sum(__uint_as_float(s[0]) + __uint_as_float(s[1]));
}
__device__ __forceinline__ float par16_sum(float x) { x += dpp<XOR2>(x); x += dpp<ROR4>(x); return x + dpp<ROR8>(x); }
__device__ __forceinline__ float bperm(int srclane, float v) { return __builtin_bit_cast(float, __builtin_amdgcn_ds_bpermute(srclane * 4, __builtin_bit_cast(int, v))); }

__device__ __forceinline__ void wait_lds_ge(unsigned addr, unsigned want) {
#pragma unroll 1
    for (unsigned i = 0; i < (1u << 22); ++i) { if ((unsigned)__builtin_amdgcn_readfirstlane(*(volatile LAS unsigned*)addr) >= want) break; __builtin_amdgcn_s_sleep(1); }
    cbar();
}
__device__ __forceinline__ void wait_prog(unsigned want) {
#pragma unroll 1
    for (unsigned i = 0; i < (1u << 22); ++i) {
        const v4u p = *(volatile LAS v4u*)L_F;
        const unsigned m01 = p.x < p.y ? p.x : p.y, m23 = p.z < p.w ? p.z : p.w;
        if ((unsigned)__builtin_amdgcn_readfirstlane(m01 < m23 ? m01 : m23) >= want) break;
        __builtin_amdgcn_s_sleep(1);
    }
    cbar();
}
__device__ __forceinline__ void lds_signal(unsigned addr) {
    asm volatile("s_waitcnt lgkmcnt(0)\n\ts_mov_b64 exec, 1\n\tds_add_u32 %0, %1\n\ts_mov_b64 exec, -1" :: "v"(addr), "v"(1u) : "memory");
}
__device__ __forceinline__ bool tags_ok(v4u g, unsigned t0, unsigned t1) { return (g.y == t0) & (g.w == t1); }
__device__ __forceinline__ v4u ldg(__amdgpu_buffer_rsrc_t rs, int off) { return __builtin_amdgcn_raw_buffer_load_b128(rs, off, 0, AUX_SC1); }
__device__ __forceinline__ void stg(__amdgpu_buffer_rsrc_t rs, int off, unsigned p0, unsigned p1, unsigned t0, unsigned t1) { __builtin_amdgcn_raw_buffer_store_b128(v4u{p0, t0, p1, t1}, rs, off, 0, AUX_SC1); }

#define STAMP(i) do { } while (0)
#define NPOLL(i, v) do { } while (0)
#define STAMP_DECL do { } while (0)
#define STAMP_FLUSH() do { } while (0)

__global__ void __launch_bounds__(512, 2) fused(Args a) {
    const int tid = threadIdx.x, lane = tid & 63, w = __builtin_amdgcn_readfirstlane(tid >> 6);
    const int blk = blockIdx.x, kvh = blk & 7, j = blk >> 3, b = j >> 3, s = j & 7;
    const bool hrole = blk < 196, owner = j < 16;
    const int own = kvh * 16 + j;
    const bool mlpo = owner && own < 112;
    const int nq = b < 2 ? 4 : 3;
    const unsigned nW = 1 + nq + (hrole ? 2 : 0);
    STAMP_DECL;
    STAMP(0);
    if (__builtin_expect(w < 4, 1)) {
        const int L = w;
        if (lane < 56) dma16<false>(lane * 16, (unsigned long long)a.x + L * 896, L_X + L * 912);
        const unsigned f = ((lane >> 4) & 1) | ((L & 1) << 1), sp = lane & 7, gp = (((sp >> 1) ^ f) << 1) | (sp & 1);
        {
            const int r0 = b < 2 ? b * 128 : 256 + (b - 2) * 96;
            unsigned long long p = (unsigned long long)a.wq + (size_t)(r0 + 8 * L) * 8192 + (kvh * 512 + s * 64) * 2; unsigned l = L_WQ + L * 1024;
            const unsigned voff = (lane >> 3) * 8192 + gp * 16;
#pragma unroll 1
            for (int i = 0; i < nq; ++i) { dma16<true>(voff, p, l); p += 32 * 8192; l += 4096; }
        }
        if (hrole) {
            const int cb = blk % 28, rb = blk / 28;
            unsigned long long p = (unsigned long long)a.w1 + (size_t)(rb * 64 + 8 * L) * 3584 + cb * 128;
            const unsigned voff = (lane >> 3) * 3584 + gp * 16;
            dma16<true>(voff, p, L_W1 + L * 1024);
            dma16<true>(voff, p + 32 * 3584, L_W1 + L * 1024 + 4096);
        }
        const size_t kvbase = ((size_t)(b * 8 + kvh) * 4096 + 3072) * 512 + (size_t)s * 65536 + L * 1024;
        unsigned n = nW;
        volatile LAS unsigned* prog = (volatile LAS unsigned*)(L_F + 4 * L);
        {
            const unsigned voff = (lane >> 5) * 512 + ((lane & 31) ^ (lane >> 5)) * 16;
            unsigned long long p = (unsigned long long)a.k + kvbase; unsigned l = L_K + L * 1056;
            dma16<true>(voff, p, l); ++n; p += 4096; l += 4224;
            __builtin_amdgcn_s_barrier();
#pragma unroll 1
            for (int i = 1; i < 16; ++i) { dma16<true>(voff, p, l); ++n; waitv<WINL - 1>(); const int d = (int)n - (WINL - 1); *prog = d > 0 ? d : 0; p += 4096; l += 4224; }
        }
        STAMP(1);
        {
            const unsigned voff = (lane >> 5) * 512 + ((lane & 31) ^ ((lane >> 5) << 1)) * 16;
            unsigned long long p = (unsigned long long)a.v + kvbase; unsigned l = L_V + L * 1088;
#pragma unroll 1
            for (int i = 0; i < 16; ++i) {
                if (i == 12) wait_lds_ge(F_S1, 4);
                dma16<true>(voff, p, l); ++n; waitv<WINL - 1>(); *prog = n - (WINL - 1); p += 4096; l += 4352;
            }
        }
        STAMP(2);
        if (owner) {
            wait_lds_ge(F_QK, 4);
            const unsigned voff = lane * 16;
            unsigned l = L_K + L * 928;
            const unsigned long long w2p = (unsigned long long)a.w2 + (unsigned)(((mlpo ? own : 0) * 16 + L) * 896);
            const unsigned long long wop = (unsigned long long)a.wo + (unsigned)((own * 32 + L) * 896);
            if (lane < 56) {
#pragma unroll
                for (int i = 0; i < 4; ++i) dma16<true>(voff, w2p + i * 4 * 896, l + i * 3712);
            }
            waitv<8>(); *prog = n - 4;
            waitv<4>(); *prog = n;
            if (lane < 56) {
#pragma unroll
                for (int i = 0; i < 8; ++i) dma16<true>(voff, wop + i * 4 * 896, l + (4 + i) * 3712);
            }
            waitv<8>(); *prog = n + 4;
            waitv<0>(); *prog = n + 12;
        } else {
            waitv<4>(); *prog = n - 4;
            waitv<0>(); *prog = n;
        }
        STAMP(3);
        STAMP_FLUSH();
        return;
    }
    const int t = w - 4, n = lane & 15, g = lane >> 4;
    __builtin_amdgcn_s_setprio(SETPRIO);
    if (t == 0) {
        if (lane < 16) *(LAS unsigned*)(L_F + 4 * lane) = 0u;
        if (lane < 56) *(LAS v4u*)(L_X + 4 * 912 + lane * 16) = v4u{0x3F803F80u, 0x3F803F80u, 0x3F803F80u, 0x3F803F80u};
    }
    asm volatile("s_waitcnt lgkmcnt(0)" ::: "memory");
    __builtin_amdgcn_s_barrier();
    cbar();
    {
        unsigned long long pc;
        asm volatile("s_getpc_b64 %0" : "=s"(pc));
        pc = (pc & ~0xFFull) + t * 3072;
        dma16<false>(lane * 16, pc, L_PF);
        dma16<false>(lane * 16, pc + 1024, L_PF);
        dma16<false>(lane * 16, pc + 2048, L_PF);
    }
    const __amdgpu_buffer_rsrc_t rs = __builtin_amdgcn_make_buffer_rsrc(a.ws, 0, WS_END, 0x00020000);
    const unsigned Eu = *(const __attribute__((address_space(4))) unsigned*)(a.ws + WS_E);
    const unsigned T0 = Eu * 0x9E3779B1u + 0x7F4A7C15u, T1 = (Eu ^ 0x5BD1E995u) * 0x85EBCA6Bu + 0x165667B1u;
    v2u b1v = v2u{0u, 0u};
    if (mlpo && lane < 32) b1v = *(const GAS v2u*)(a.b1 + (own * 16 + (lane >> 3) * 4) * 2);
    const int fb = blk / 56, fc = blk % 56;
    v2u xres = v2u{0u, 0u};
    if (t == 3 && blk < 224 && lane < 2) xres = *(const GAS v2u*)(a.x + (fb * 448 + 8 * fc + 4 * lane) * 2);
    const unsigned r1 = n >> 2, c4 = n & 3;

    {
        const unsigned xa = L_X + (n < 4 ? n : 4) * 912 + g * 16;
        const unsigned fs = ((r1 >> 1) & 1) | ((g & 1) << 1);
        const unsigned wo_ = (8 * g + r1) * 128 + ((t ^ fs) * 32) + c4 * 8;
        const int ks0 = b < 2 ? b * 4 : 8 + (b - 2) * 3, rb = blk / 28;
        const int qoff = WS_Q + (((n * 8 + kvh) * 4 + b) * 128 + s * 16 + t * 4 + g) * 16;
        const int hoff = WS_H + ((n * 448 + (blk % 28) * 16 + t * 4 + g) * 8 + rb) * 16;
        wait_prog(1);
        v4u xf[14];
#pragma unroll
        for (int ks = 0; ks < 14; ++ks) xf[ks] = lds128(xa + 64 * ks);
        v4u bq[4], bh[2];
#pragma unroll
        for (int kk = 0; kk < 4; ++kk) bq[kk] = lds128(xa + 64 * (ks0 + (kk < nq ? kk : 0)));
#pragma unroll
        for (int kk = 0; kk < 2; ++kk) bh[kk] = lds128(xa + 64 * ((hrole ? 2 * rb : 0) + kk));
        __builtin_amdgcn_sched_barrier(0);
        f32x4 G0 = {0.f, 0.f, 0.f, 0.f}, G1 = {0.f, 0.f, 0.f, 0.f};
#pragma unroll
        for (int ks = 0; ks < 14; ks += 2) { G0 = mfma(xf[ks], xf[ks], G0); G1 = mfma(xf[ks + 1], xf[ks + 1], G1); }
        const f32x4 G = G0 + G1;
        const float gsel = n == 0 ? G[0] : n == 1 ? G[1] : n == 2 ? G[2] : G[3];
        const float sq = bperm(n & 3, gsel), sm = bperm(16 + (n & 3), G[0]);
        const float mean = sm * (1.0f / 448.0f);
        const float var = fmaxf(fmaf(sq, 1.0f / 448.0f, -mean * mean), 0.f);
        const float rstd = __builtin_amdgcn_rsqf(var + 1e-5f), mr = mean * rstd;
        asm volatile("" :: "v"(rstd), "v"(mr), "v"(qoff), "v"(hoff));
        wait_prog(nW);
        STAMP(1);
        v4u aq[4], ah[2];
#pragma unroll
        for (int kk = 0; kk < 4; ++kk) aq[kk] = trfrag(L_WQ + wo_ + kk * 4096, L_WQ + wo_ + kk * 4096 + 512);
#pragma unroll
        for (int kk = 0; kk < 2; ++kk) ah[kk] = trfrag(L_W1 + wo_ + kk * 4096, L_W1 + wo_ + kk * 4096 + 512);
        __builtin_amdgcn_sched_barrier(0);
        f32x4 Dq = {0.f, 0.f, 0.f, 0.f}, Dq2 = {0.f, 0.f, 0.f, 0.f}, Dh = {0.f, 0.f, 0.f, 0.f};
        Dq = mfma(aq[0], bq[0], Dq);
        Dq2 = mfma(aq[1], bq[1], Dq2);
        Dh = mfma(ah[0], bh[0], Dh);
        Dq = mfma(aq[2], bq[2], Dq);
        if (nq == 4) Dq2 = mfma(aq[3], bq[3], Dq2);
        Dh = mfma(ah[1], bh[1], Dh);
        Dq += Dq2;
        lds_signal(F_S1);
        float q[4], h[4];
#pragma unroll
        for (int r = 0; r < 4; ++r) { q[r] = fmaf(rstd, Dq[r], -mr * dpp<NBC4>(Dq[r])); h[r] = fmaf(rstd, Dh[r], -mr * dpp<NBC4>(Dh[r])); }
        if (n < 4) {
            stg(rs, qoff, cvtpk(q[0], q[1]), cvtpk(q[2], q[3]), T0, T1);
            if (hrole) stg(rs, hoff, cvtpk(h[0], h[1]), cvtpk(h[2], h[3]), T0, T1);
        }
    }
    STAMP(2);
    const int gid = b * 8 + kvh;
    v4u hx = v4u{0u, 0u, 0u, 0u};
    const bool hval = mlpo && lane < 32 && (lane & 7) < 7;
    {
        const int qo = WS_Q + (gid * 512 + (lane >> 5) * 128 + 32 * t + (lane & 31)) * 16;
        const int ho = mlpo ? WS_H + (t * 448 + own * 4) * 128 + (lane & 31) * 16 : qo;
        v4u x0, x1;
        wait_prog(nW + QPOLL);
#pragma unroll 1
        for (unsigned it = 0; it < SPIN_CAP; ++it) {
            cbar();
            NPOLL(9, 1);
            x0 = ldg(rs, qo); x1 = ldg(rs, qo + 256 * 16); hx = ldg(rs, ho);
            if (__all(tags_ok(x0, T0, T1) & tags_ok(x1, T0, T1) & (!hval | tags_ok(hx, T0, T1)))) break;
        }
        STAMP(3);
        const float q0 = x32_sum(bflo(x0.x) + bflo(x1.x)), q1 = x32_sum(bfhi(x0.x) + bfhi(x1.x));
        const float q2 = x32_sum(bflo(x0.z) + bflo(x1.z)), q3 = x32_sum(bfhi(x0.z) + bfhi(x1.z));
        if (lane < 32) *(LAS v2u*)(L_Q + (32 * t + lane) * 8) = v2u{cvtpk(q0, q1), cvtpk(q2, q3)};
        lds_signal(F_Q);
    }
    float mW, lW;
    {
        const unsigned ka = L_K + t * 16896 + (n >> 1) * 1056 + (n & 1) * 512 + ((g ^ (n & 1)) * 16);
        wait_prog(nW + 4 * (t + 1));
        v4u kf[16];
#pragma unroll
        for (int i = 0; i < 16; ++i) kf[i] = lds128(ka + (i >> 3) * 8448 + (i & 7) * 64);
        wait_lds_ge(F_Q, 4);
        v4u qf[8];
#pragma unroll
        for (int ks = 0; ks < 8; ++ks) qf[ks] = lds128(L_Q + (n & 1) * 512 + ks * 64 + g * 16);
        __builtin_amdgcn_sched_barrier(0);
        f32x4 z0 = {0.f, 0.f, 0.f, 0.f}, z1 = {0.f, 0.f, 0.f, 0.f};
#pragma unroll
        for (int ks = 0; ks < 8; ++ks) { z0 = mfma(kf[ks], qf[ks], z0); z1 = mfma(kf[8 + ks], qf[ks], z1); }
        lds_signal(F_QK);
        constexpr float C = 0.0625f * 1.4426950408889634f;
        const float z[4] = {(n & 2) ? z1[0] : z0[0], (n & 2) ? z1[1] : z0[1], (n & 2) ? z1[2] : z0[2], (n & 2) ? z1[3] : z0[3]};
        float m = fmaxf(fmaxf(z[0], z[1]), fmaxf(z[2], z[3])) * C;
        m = xrow_max(fmaxf(m, dpp<XOR2>(m)));
        if (lane < 2) *(LAS float*)(L_MX + lane * 16 + t * 4) = m;
        lds_signal(F_M);
        wait_lds_ge(F_M, 4);
        const v4u mx = lds128(L_MX + (n & 1) * 16);
        mW = fmaxf(fmaxf(__uint_as_float(mx.x), __uint_as_float(mx.y)), fmaxf(__uint_as_float(mx.z), __uint_as_float(mx.w)));
        float p[4];
#pragma unroll
        for (int r = 0; r < 4; ++r) p[r] = __builtin_amdgcn_exp2f(fmaf(z[r], C, -mW));
        float l = (p[0] + p[1]) + (p[2] + p[3]);
        l = xrow_sum(l + dpp<XOR2>(l));
        if (n < 4) *(LAS v2u*)(L_P + (n & 1) * 256 + (32 * t + 8 * g + 4 * (n >> 1)) * 2) = v2u{cvtpk(p[0], p[1]), cvtpk(p[2], p[3])};
        if (lane < 2) *(LAS float*)(L_LS + lane * 16 + t * 4) = l;
        lds_signal(F_P);
    }
    STAMP(4);
    if (mlpo) {
        const float hv[4] = {hval ? bflo(hx.x) : 0.f, hval ? bfhi(hx.x) : 0.f, hval ? bflo(hx.z) : 0.f, hval ? bfhi(hx.z) : 0.f};
        const float bb[4] = {bflo(b1v.x), bfhi(b1v.x), bflo(b1v.y), bfhi(b1v.y)};
        float act[4];
#pragma unroll
        for (int r = 0; r < 4; ++r) {
            float v = hv[r]; v += dpp<XOR1>(v); v += dpp<XOR2>(v); v += dpp<HMIR>(v); v += bb[r];
            const float e = __builtin_amdgcn_exp2f(-1.4426950408889634f * fabsf(v));
            const float num = v > 0.f ? fmaf(2.f, e, 1.f) : e * (e + 2.f);
            const float den = v > 0.f ? fmaf(2.f * e, e, num) : num + 2.f;
            act[r] = v * num * __builtin_amdgcn_rcpf(den);
        }
        if (lane < 32 && (lane & 7) == 0) *(LAS v2u*)(L_AM + t * 32 + (lane >> 3) * 8) = v2u{cvtpk(act[0], act[1]), cvtpk(act[2], act[3])};
        lds_signal(F_AM);
    }
    {
        const unsigned e = r1 & 1;
        const unsigned vb = L_V + (2 * g + (r1 >> 1)) * 1088 + e * 512 + t * 128 + c4 * 8;
        const unsigned va0 = vb + e * 32, va1 = vb + (e ^ 1) * 32;
        const int ooff = WS_O + ((((kvh * 16 + (n & 1) * 8 + 2 * t + ((n >> 2) & 1)) * 4 + b) * 8 + s) * 8 + 4 * ((n >> 1) & 1) + g) * 16;
        const int soff = WS_S + (((kvh * 2 + (lane & 1)) * 4 + b) * 8 + s) * 16;
        wait_lds_ge(F_P, 4);
        v4u pf[4];
#pragma unroll
        for (int kk = 0; kk < 4; ++kk) pf[kk] = lds128(L_P + (n & 1) * 256 + kk * 64 + g * 16);
        const v4u ls = lds128(L_LS + (n & 1) * 16);
        lW = (__uint_as_float(ls.x) + __uint_as_float(ls.y)) + (__uint_as_float(ls.z) + __uint_as_float(ls.w));
        if (t == 0 && lane < 2) stg(rs, soff, __float_as_uint(mW), __float_as_uint(lW), T0, T1);
        f32x4 o[4];
#pragma unroll
        for (int aa = 0; aa < 4; ++aa) o[aa] = f32x4{0.f, 0.f, 0.f, 0.f};
#pragma unroll
        for (int kk = 0; kk < 4; ++kk) {
            wait_prog(nW + 16 + 4 * (kk + 1));
            v4u vf[4];
#pragma unroll
            for (int aa = 0; aa < 4; ++aa) { const unsigned ad = ((aa & 1) ? va1 : va0) + (aa >> 1) * 64 + kk * 17408; vf[aa] = trfrag(ad, ad + 8704); }
#pragma unroll
            for (int aa = 0; aa < 4; ++aa) o[aa] = mfma(vf[aa], pf[kk], o[aa]);
        }
        float v[4];
#pragma unroll
        for (int r = 0; r < 4; ++r) { const float lo = (n & 2) ? o[1][r] : o[0][r], hi = (n & 2) ? o[3][r] : o[2][r]; v[r] = (n & 4) ? hi : lo; }
        if (n < 8) stg(rs, ooff, cvtpk(v[0], v[1]), cvtpk(v[2], v[3]), T0, T1);
    }
    STAMP(5);
    if (owner) {
        const unsigned ba = L_K + (4 * g + r1) * 928 + t * 224 + c4 * 8;
        f32x4 acc[7];
        v4u wf[7];
        wait_prog(nW + 32 + 4);
        if (mlpo) {
            wait_lds_ge(F_AM, 4);
            const v2u am = lds64(L_AM + (n & 3) * 32 + g * 8);
            const v4u B = v4u{am.x, am.y, 0u, 0u};
#pragma unroll
            for (int T = 0; T < 7; ++T) { const v2u wl = tr64(ba + T * 32); acc[T] = mfma(v4u{wl.x, wl.y, 0u, 0u}, B, f32x4{0.f, 0.f, 0.f, 0.f}); }
        } else {
#pragma unroll
            for (int T = 0; T < 7; ++T) acc[T] = f32x4{0.f, 0.f, 0.f, 0.f};
        }
        const int h = j >> 3;
        const int oo = WS_O + ((own * 4 + t) * 64 + lane) * 16, so = WS_S + (((kvh * 2 + h) * 4 + t) * 8 + (lane >> 3)) * 16;
        const int foff0 = WS_FIN + ((((n & 3) * 56 + 2 * (7 * t + (n >> 2)) + (g >> 1)) * 128 + own) * 2 + (g & 1)) * 16;
        wait_prog(nW + 32 + 12);
#pragma unroll
        for (int T = 0; T < 7; ++T) wf[T] = trfrag(ba + 16 * 928 + T * 32, ba + 32 * 928 + T * 32);
        v4u og, sg;
#pragma unroll 1
        for (unsigned it = 0; it < SPIN_CAP; ++it) {
            cbar();
            og = ldg(rs, oo); sg = ldg(rs, so);
            if (__all(tags_ok(og, T0, T1) & tags_ok(sg, T0, T1))) break;
        }
        STAMP(6);
        const float m = __uint_as_float(sg.x), l = __uint_as_float(sg.z);
        const float M = xrow_max(fmaxf(m, dpp<ROR8>(m))), e = __builtin_amdgcn_exp2f(m - M);
        float le = l * e; le += dpp<ROR8>(le);
        const float wgt = e * __builtin_amdgcn_rcpf(xrow_sum(le));
        float v[4] = {bflo(og.x) * wgt, bfhi(og.x) * wgt, bflo(og.z) * wgt, bfhi(og.z) * wgt};
#pragma unroll
        for (int r = 0; r < 4; ++r) { v[r] += dpp<ROR8>(v[r]); v[r] = xrow_sum(v[r]); }
        if (lane < 8) *(LAS v2u*)(L_A + t * 64 + (8 * (lane & 3) + 4 * (lane >> 2)) * 2) = v2u{cvtpk(v[0], v[1]), cvtpk(v[2], v[3])};
        lds_signal(F_A);
        wait_lds_ge(F_A, 4);
        const v4u B = lds128(L_A + (n & 3) * 64 + g * 16);
#pragma unroll
        for (int T = 0; T < 7; ++T) acc[T] = mfma(wf[T], B, acc[T]);
        unsigned p0[7], p1[7];
#pragma unroll
        for (int T = 0; T < 7; ++T) { p0[T] = cvtpk(acc[T][0], acc[T][1]); p1[T] = cvtpk(acc[T][2], acc[T][3]); }
        const int q4 = n >> 2;
        const unsigned a0 = q4 == 0 ? p0[0] : q4 == 1 ? p0[1] : q4 == 2 ? p0[2] : p0[3], a1 = q4 == 0 ? p1[0] : q4 == 1 ? p1[1] : q4 == 2 ? p1[2] : p1[3];
        const unsigned c0 = q4 == 0 ? p0[4] : q4 == 1 ? p0[5] : p0[6], c1 = q4 == 0 ? p1[4] : q4 == 1 ? p1[5] : p1[6];
        stg(rs, foff0, a0, a1, T0, T1);
        if (q4 < 3) stg(rs, foff0 + 8 * 128 * 2 * 16, c0, c1, T0, T1);
    }
    STAMP(7);
    if (t == 3 && blk < 224) {
        v4u f0, f1, f2, f3;
        const int fo = WS_FIN + ((fb * 56 + fc) * 256 + lane) * 16;
        {
            const int pb = WS_FIN + ((fb * 56 + fc) * 256 + (lane & 7) * 32 + 30) * 16;
#pragma unroll 1
            for (unsigned it = 0; it < SPIN_CAP; ++it) {
                cbar();
                bool ok = true;
                if (lane < 8) ok = tags_ok(ldg(rs, pb), T0, T1);
                if (__all(ok)) break;
                __builtin_amdgcn_s_sleep(FPROBE);
            }
        }
#pragma unroll 1
        for (unsigned it = 0; it < SPIN_CAP; ++it) {
            cbar();
            f0 = ldg(rs, fo); f1 = ldg(rs, fo + 1024); f2 = ldg(rs, fo + 2048); f3 = ldg(rs, fo + 3072);
            if (__all(tags_ok(f0, T0, T1) & tags_ok(f1, T0, T1) & tags_ok(f2, T0, T1) & tags_ok(f3, T0, T1))) break;
        }
        float v[4] = {(bflo(f0.x) + bflo(f1.x)) + (bflo(f2.x) + bflo(f3.x)), (bfhi(f0.x) + bfhi(f1.x)) + (bfhi(f2.x) + bfhi(f3.x)),
                      (bflo(f0.z) + bflo(f1.z)) + (bflo(f2.z) + bflo(f3.z)), (bfhi(f0.z) + bfhi(f1.z)) + (bfhi(f2.z) + bfhi(f3.z))};
#pragma unroll
        for (int r = 0; r < 4; ++r) v[r] = xrow_sum(par16_sum(v[r]));
        if (lane < 2) {
            asm volatile("" : "+v"(xres));
            *(GAS v2u*)(a.out + fb * 448 + 8 * fc + 4 * lane) = v2u{cvtpk(bflo(xres.x) + v[0], bfhi(xres.x) + v[1]), cvtpk(bflo(xres.y) + v[2], bfhi(xres.y) + v[3])};
        }
        if (blk == 0 && lane == 0) *(GAS unsigned*)(a.ws + WS_E) = Eu + 1u;
        STAMP(8);
    }
    STAMP_FLUSH();
}
}

extern "C" void kernel_launch(void* const* d_in, const int* in_sizes, int n_in, void* d_out, int out_size, void* d_ws, size_t ws_size, hipStream_t stream) {
    (void)in_sizes; (void)n_in; (void)out_size; (void)ws_size;
    fk::Args a;
    a.x = (const unsigned char*)d_in[0]; a.k = (const unsigned char*)d_in[1]; a.v = (const unsigned char*)d_in[2];
    a.wq = (const unsigned char*)d_in[3]; a.wo = (const unsigned char*)d_in[4]; a.w1 = (const unsigned char*)d_in[6];
    a.b1 = (const unsigned char*)d_in[7]; a.w2 = (const unsigned char*)d_in[8]; a.out = (unsigned short*)d_out; a.ws = (unsigned char*)d_ws;
    (void)hipFuncSetAttribute((const void*)fk::fused, hipFuncAttributeMaxDynamicSharedMemorySize, fk::LDS_BYTES);
    hipLaunchKernelGGL(fk::fused, dim3(256), dim3(512), fk::LDS_BYTES, stream, a);
}
```

```cpp
#include <hip/hip_runtime.h>
#include <hip/hip_bf16.h>
#include <stdint.h>

#define GAS __attribute__((address_space(1)))
#define LAS __attribute__((address_space(3)))
typedef unsigned v4u __attribute__((ext_vector_type(4)));
typedef unsigned v2u __attribute__((ext_vector_type(2)));
typedef float f32x4 __attribute__((ext_vector_type(4)));
typedef float f32x2 __attribute__((ext_vector_type(2)));
typedef short s16x4 __attribute__((ext_vector_type(4)));
typedef __bf16 bf16x8_t __attribute__((ext_vector_type(8)));
typedef __bf16 bf16x2_t __attribute__((ext_vector_type(2)));

#define STAMPS 0
#define SETPRIO 3
#define QPOLL 10
#define FPROBE 6
#define WINK 6
#define WINSW 4
#define WINL 8
#define PREFETCH_CODE 1

namespace fk {
constexpr int AUX_SC1 = 16;
constexpr int L_K = 0;
constexpr int L_WQ = 67584;
constexpr int L_V = L_WQ + 16384;
constexpr int L_X = L_V + 69632 - 8192 - 4608;
constexpr int L_W1 = L_V + 69632 - 8192;
constexpr int L_M = L_V + 69632;
constexpr int L_Q = L_M;
constexpr int L_P = L_Q + 1024;
constexpr int L_A = L_P + 512;
constexpr int L_AM = L_A + 256;
constexpr int L_MX = L_AM + 128;
constexpr int L_LS = L_MX + 32;
constexpr int L_F = L_LS + 32;
constexpr int F_S1 = L_F + 16, F_QK = L_F + 20, F_M = L_F + 24, F_P = L_F + 28, F_Q = L_F + 32, F_A = L_F + 36, F_AM = L_F + 40;
constexpr int L_PF = L_F + 64;
constexpr int LDS_BYTES = L_PF + 1024;
static_assert(L_X >= L_V + 52 * 1088, "X / W1 images alias V instructions >= 52 only");
static_assert(48 * 928 <= 67584, "projection rows inside the K image");
static_assert(LDS_BYTES <= 163840, "LDS");
static_assert((L_F & 15) == 0 && (L_MX & 15) == 0 && (L_A & 15) == 0, "alignment");
constexpr int WS_E = 0;
constexpr int WS_Q = 4096;
constexpr int WS_H = WS_Q + 4 * 8 * 4 * 128 * 16;
constexpr int WS_O = WS_H + 4 * 7 * 448 * 16;
constexpr int WS_S = WS_O + 128 * 256 * 16;
constexpr int WS_FIN = WS_S + 512 * 64;
constexpr int WS_ST = WS_FIN + 4 * 28 * 512 * 16;
constexpr int WS_END = WS_ST + 256 * 8 * 16 * 8;
constexpr unsigned SPIN_CAP = 1u << 17;

struct Args {
    const unsigned char* x; const unsigned char* k; const unsigned char* v; const unsigned char* wq; const unsigned char* wo;
    const unsigned char* w1; const unsigned char* b1; const unsigned char* w2; unsigned short* out; unsigned char* ws;
};

__device__ __forceinline__ unsigned long long rt() { return __builtin_amdgcn_s_memrealtime(); }
template <bool NT> __device__ __forceinline__ void dma16(unsigned voff, unsigned long long sbase, unsigned lds) {
    if (NT) asm volatile("s_mov_b32 m0, %2\n\ts_nop 0\n\tglobal_load_lds_dwordx4 %0, %1 nt" :: "v"(voff), "s"(sbase), "s"(lds) : "memory");
    else    asm volatile("s_mov_b32 m0, %2\n\ts_nop 0\n\tglobal_load_lds_dwordx4 %0, %1" :: "v"(voff), "s"(sbase), "s"(lds) : "memory");
}
template <int N> __device__ __forceinline__ void waitv() { asm volatile("s_waitcnt vmcnt(%0)" :: "i"(N) : "memory"); }
__device__ __forceinline__ void cbar() { asm volatile("" ::: "memory"); }

__device__ __forceinline__ f32x4 mfma(v4u a, v4u b, f32x4 c) {
    return __builtin_amdgcn_mfma_f32_16x16x32_bf16(__builtin_bit_cast(bf16x8_t, a), __builtin_bit_cast(bf16x8_t, b), c, 0, 0, 0);
}
__device__ __forceinline__ v4u lds128(unsigned a) { return *(const LAS v4u*)a; }
__device__ __forceinline__ v2u lds64(unsigned a) { return *(const LAS v2u*)a; }
__device__ __forceinline__ v2u tr64(unsigned a) { return __builtin_bit_cast(v2u, __builtin_amdgcn_ds_read_tr16_b64_v4i16((LAS s16x4*)a)); }
__device__ __forceinline__ v4u trfrag(unsigned a0, unsigned a1) { v2u lo = tr64(a0), hi = tr64(a1); return v4u{lo.x, lo.y, hi.x, hi.y}; }
__device__ __forceinline__ unsigned cvtpk(float lo, float hi) { return __builtin_bit_cast(unsigned, __builtin_convertvector(f32x2{lo, hi}, bf16x2_t)); }
__device__ __forceinline__ float bflo(unsigned u) { return __builtin_bit_cast(float, u << 16); }
__device__ __forceinline__ float bfhi(unsigned u) { return __builtin_bit_cast(float, u & 0xffff0000u); }
template <int CTRL> __device__ __forceinline__ float dpp(float x) {
    return __builtin_bit_cast(float, __builtin_amdgcn_mov_dpp(__builtin_bit_cast(int, x), CTRL, 0xf, 0xf, true));
}
constexpr int XOR1 = 0xB1, XOR2 = 0x4E, HMIR = 0x141, ROR4 = 0x124, ROR8 = 0x128, NBC4 = 0x154;
__device__ __forceinline__ float xrow_max(float x) {
    auto s = __builtin_amdgcn_permlane16_swap(__float_as_uint(x), __float_as_uint(x), false, false);
    x = fmaxf(__uint_as_float(s[0]), __uint_as_float(s[1]));
    auto t = __builtin_amdgcn_permlane32_swap(__float_as_uint(x), __float_as_uint(x), false, false);
    return fmaxf(__uint_as_float(t[0]), __uint_as_float(t[1]));
}
__device__ __forceinline__ float x32_sum(float x) {
    auto t = __builtin_amdgcn_permlane32_swap(__float_as_uint(x), __float_as_uint(x), false, false);
    return __uint_as_float(t[0]) + __uint_as_float(t[1]);
}
__device__ __forceinline__ float x16_sum(float x) {
    auto s = __builtin_amdgcn_permlane16_swap(__float_as_uint(x), __float_as_uint(x), false, false);
    return __uint_as_float(s[0]) + __uint_as_float(s[1]);
}
__device__ __forceinline__ float xrow_sum(float x) {
    auto s = __builtin_amdgcn_permlane16_swap(__float_as_uint(x), __float_as_uint(x), false, false);
    return x32_sum(__uint_as_float(s[0]) + __uint_as_float(s[1]));
}
__device__ __forceinline__ float par16_sum(float x) { x += dpp<XOR2>(x); x += dpp<ROR4>(x); return x + dpp<ROR8>(x); }
__device__ __forceinline__ float bperm(int srclane, float v) { return __builtin_bit_cast(float, __builtin_amdgcn_ds_bpermute(srclane * 4, __builtin_bit_cast(int, v))); }

__device__ __forceinline__ void wait_lds_ge(unsigned addr, unsigned want) {
#pragma unroll 1
    for (unsigned i = 0; i < (1u << 22); ++i) { if ((unsigned)__builtin_amdgcn_readfirstlane(*(volatile LAS unsigned*)addr) >= want) break; __builtin_amdgcn_s_sleep(1); }
    cbar();
}
__device__ __forceinline__ void spin_lds_ge(unsigned addr, unsigned want) {
#pragma unroll 1
    for (unsigned i = 0; i < (1u << 24); ++i) { if ((unsigned)__builtin_amdgcn_readfirstlane(*(volatile LAS unsigned*)addr) >= want) break; }
    cbar();
}
__device__ __forceinline__ void wait_prog(unsigned want) {
#pragma unroll 1
    for (unsigned i = 0; i < (1u << 22); ++i) {
        const v4u p = *(volatile LAS v4u*)L_F;
        const unsigned m01 = p.x < p.y ? p.x : p.y, m23 = p.z < p.w ? p.z : p.w;
        if ((unsigned)__builtin_amdgcn_readfirstlane(m01 < m23 ? m01 : m23) >= want) break;
        __builtin_amdgcn_s_sleep(1);
    }
    cbar();
}
__device__ __forceinline__ void wait_prog_c(unsigned want, unsigned& seen) {
#pragma unroll 1
    for (unsigned i = 0; i < (1u << 24) && seen < want; ++i) {
        const v4u p = *(volatile LAS v4u*)L_F;
        const unsigned m01 = p.x < p.y ? p.x : p.y, m23 = p.z < p.w ? p.z : p.w;
        seen = (unsigned)__builtin_amdgcn_readfirstlane(m01 < m23 ? m01 : m23);
    }
    cbar();
}
__device__ __forceinline__ void lds_signal(unsigned addr) {
    asm volatile("s_waitcnt lgkmcnt(0)\n\ts_mov_b64 exec, 1\n\tds_add_u32 %0, %1\n\ts_mov_b64 exec, -1" :: "v"(addr), "v"(1u) : "memory");
}
__device__ __forceinline__ bool tags_ok(v4u g, unsigned t0, unsigned t1) { return (g.y == t0) & (g.w == t1); }
__device__ __forceinline__ v4u ldg(__amdgpu_buffer_rsrc_t rs, int off) { return __builtin_amdgcn_raw_buffer_load_b128(rs, off, 0, AUX_SC1); }
__device__ __forceinline__ void stg(__amdgpu_buffer_rsrc_t rs, int off, unsigned p0, unsigned p1, unsigned t0, unsigned t1) { __builtin_amdgcn_raw_buffer_store_b128(v4u{p0, t0, p1, t1}, rs, off, 0, AUX_SC1); }

#define STAMP(i) do { } while (0)
#define NPOLL(i, v) do { } while (0)
#define STAMP_DECL do { } while (0)
#define STAMP_FLUSH() do { } while (0)

__global__ void __launch_bounds__(512, 2) fused(Args a) {
    const int tid = threadIdx.x, lane = tid & 63, w = __builtin_amdgcn_readfirstlane(tid >> 6);
    const int blk = blockIdx.x, kvh = blk & 7, j = blk >> 3, b = j >> 3, s = j & 7;
    const bool hrole = blk < 196, owner = j < 16;
    const int own = kvh * 16 + j;
    const bool mlpo = owner && own < 112;
    const int nq = b < 2 ? 4 : 3;
    const unsigned nW = 1 + nq + (hrole ? 2 : 0);
    STAMP_DECL;
    STAMP(0);
    if (__builtin_expect(w < 4, 1)) {
        const int L = w;
        if (lane < 56) dma16<false>(lane * 16, (unsigned long long)a.x + L * 896, L_X + L * 912);
        const unsigned f = ((lane >> 4) & 1) | ((L & 1) << 1), sp = lane & 7, gp = (((sp >> 1) ^ f) << 1) | (sp & 1);
        {
            const int r0 = b < 2 ? b * 128 : 256 + (b - 2) * 96;
            unsigned long long p = (unsigned long long)a.wq + (size_t)(r0 + 8 * L) * 8192 + (kvh * 512 + s * 64) * 2; unsigned l = L_WQ + L * 1024;
            const unsigned voff = (lane >> 3) * 8192 + gp * 16;
#pragma unroll 1
            for (int i = 0; i < nq; ++i) { dma16<true>(voff, p, l); p += 32 * 8192; l += 4096; }
        }
        if (hrole) {
            const int cb = blk % 28, rb = blk / 28;
            unsigned long long p = (unsigned long long)a.w1 + (size_t)(rb * 64 + 8 * L) * 3584 + cb * 128;
            const unsigned voff = (lane >> 3) * 3584 + gp * 16;
            dma16<true>(voff, p, L_W1 + L * 1024);
            dma16<true>(voff, p + 32 * 3584, L_W1 + L * 1024 + 4096);
        }
        const size_t kvbase = ((size_t)(b * 8 + kvh) * 4096 + 3072) * 512 + (size_t)s * 65536 + L * 1024;
        unsigned n = nW;
        volatile LAS unsigned* prog = (volatile LAS unsigned*)(L_F + 4 * L);
        {
            const unsigned voff = (lane >> 5) * 512 + ((lane & 31) ^ (lane >> 5)) * 16;
            unsigned long long p = (unsigned long long)a.k + kvbase; unsigned l = L_K + L * 1056;
            dma16<true>(voff, p, l); ++n; p += 4096; l += 4224;
            __builtin_amdgcn_s_barrier();
#pragma unroll 1
            for (int i = 1; i < 16; ++i) { dma16<true>(voff, p, l); ++n; waitv<WINK - 1>(); const int d = (int)n - (WINK - 1); *prog = d > 0 ? d : 0; p += 4096; l += 4224; }
        }
        STAMP(1);
        {
            const unsigned voff = (lane >> 5) * 512 + ((lane & 31) ^ ((lane >> 5) << 1)) * 16;
            unsigned long long p = (unsigned long long)a.v + kvbase; unsigned l = L_V + L * 1088;
#pragma unroll 1
            for (int i = 0; i < 16; ++i) {
                if (i == 12) wait_lds_ge(F_S1, 4);
                dma16<true>(voff, p, l); ++n;
                if (i < WINSW) { waitv<WINK - 1>(); *prog = n - (WINK - 1); } else { waitv<WINL - 1>(); *prog = n - (WINL - 1); }
                p += 4096; l += 4352;
            }
        }
        STAMP(2);
        if (owner) {
            wait_lds_ge(F_P, 4);
            const unsigned voff = lane * 16;
            unsigned l = L_K + L * 928;
            const unsigned long long w2p = (unsigned long long)a.w2 + (unsigned)(((mlpo ? own : 0) * 16 + L) * 896);
            const unsigned long long wop = (unsigned long long)a.wo + (unsigned)((own * 32 + L) * 896);
            if (lane < 56) {
#pragma unroll
                for (int i = 0; i < 4; ++i) dma16<true>(voff, w2p + i * 4 * 896, l + i * 3712);
            }
            waitv<8>(); *prog = n - 4;
            waitv<4>(); *prog = n;
            STAMP(4);
            if (lane < 56) {
#pragma unroll
                for (int i = 0; i < 8; ++i) dma16<true>(voff, wop + i * 4 * 896, l + (4 + i) * 3712);
            }
            waitv<8>(); *prog = n + 4;
            waitv<0>(); *prog = n + 12;
        } else {
            waitv<4>(); *prog = n - 4;
            waitv<0>(); *prog = n;
            STAMP(4);
        }
        STAMP(3);
        STAMP_FLUSH();
        return;
    }
    const int t = w - 4, n = lane & 15, g = lane >> 4;
    __builtin_amdgcn_s_setprio(SETPRIO);
    if (t == 0) {
        if (lane < 16) *(LAS unsigned*)(L_F + 4 * lane) = 0u;
        if (lane < 56) *(LAS v4u*)(L_X + 4 * 912 + lane * 16) = v4u{0x3F803F80u, 0x3F803F80u, 0x3F803F80u, 0x3F803F80u};
    }
    asm volatile("s_waitcnt lgkmcnt(0)" ::: "memory");
    __builtin_amdgcn_s_barrier();
    cbar();
    {
        unsigned long long pc;
        asm volatile("s_getpc_b64 %0" : "=s"(pc));
        pc = (pc & ~0xFFull) + t * 3072;
        dma16<false>(lane * 16, pc, L_PF);
        dma16<false>(lane * 16, pc + 1024, L_PF);
        dma16<false>(lane * 16, pc + 2048, L_PF);
    }
    const __amdgpu_buffer_rsrc_t rs = __builtin_amdgcn_make_buffer_rsrc(a.ws, 0, WS_END, 0x00020000);
    const unsigned Eu = *(const __attribute__((address_space(4))) unsigned*)(a.ws + WS_E);
    const unsigned T0 = (Eu * 0x9E3779B1u + 0x7F4A7C15u) & 0xBFFFBFFFu, T1 = ((Eu ^ 0x5BD1E995u) * 0x85EBCA6Bu + 0x165667B1u) & 0xBFFFBFFFu;
    v2u b1v = v2u{0u, 0u};
    if (mlpo && lane < 32) b1v = *(const GAS v2u*)(a.b1 + (own * 16 + (lane & 3) * 4) * 2);
    const int fi = blk - 128, fb = fi / 28, fc = fi % 28;
    const bool fin = fi >= 0 && fi < 112;
    v2u xres = v2u{0u, 0u};
    if (t == 3 && fin && lane < 4) xres = *(const GAS v2u*)(a.x + (fb * 448 + 16 * fc + 4 * lane) * 2);
    const unsigned r1 = n >> 2, c4 = n & 3;

    {
        const unsigned xa = L_X + (n < 4 ? n : 4) * 912 + g * 16;
        const unsigned fs = ((r1 >> 1) & 1) | ((g & 1) << 1);
        const unsigned wo_ = (8 * g + r1) * 128 + ((t ^ fs) * 32) + c4 * 8;
        const int ks0 = b < 2 ? b * 4 : 8 + (b - 2) * 3, rb = blk / 28;
        const int qoff = WS_Q + (((n * 8 + kvh) * 4 + b) * 128 + s * 16 + t * 4 + g) * 16;
        const int hoff = WS_H + ((n * 7 + rb) * 448 + (blk % 28) * 16 + t * 4 + g) * 16;
        wait_prog(1);
        v4u xf[14];
#pragma unroll
        for (int ks = 0; ks < 14; ++ks) xf[ks] = lds128(xa + 64 * ks);
        v4u bq[4], bh[2];
#pragma unroll
        for (int kk = 0; kk < 4; ++kk) bq[kk] = lds128(xa + 64 * (ks0 + (kk < nq ? kk : 0)));
#pragma unroll
        for (int kk = 0; kk < 2; ++kk) bh[kk] = lds128(xa + 64 * ((hrole ? 2 * rb : 0) + kk));
        __builtin_amdgcn_sched_barrier(0);
        f32x4 G0 = {0.f, 0.f, 0.f, 0.f}, G1 = {0.f, 0.f, 0.f, 0.f};
#pragma unroll
        for (int ks = 0; ks < 14; ks += 2) { G0 = mfma(xf[ks], xf[ks], G0); G1 = mfma(xf[ks + 1], xf[ks + 1], G1); }
        const f32x4 G = G0 + G1;
        const float gsel = n == 0 ? G[0] : n == 1 ? G[1] : n == 2 ? G[2] : G[3];
        const float sq = bperm(n & 3, gsel), sm = bperm(16 + (n & 3), G[0]);
        const float mean = sm * (1.0f / 448.0f);
        const float var = fmaxf(fmaf(sq, 1.0f / 448.0f, -mean * mean), 0.f);
        const float rstd = __builtin_amdgcn_rsqf(var + 1e-5f), mr = mean * rstd;
        asm volatile("" :: "v"(rstd), "v"(mr), "v"(qoff), "v"(hoff));
        wait_prog(nW);
        STAMP(1);
        v4u aq[4], ah[2];
#pragma unroll
        for (int kk = 0; kk < 4; ++kk) aq[kk] = trfrag(L_WQ + wo_ + kk * 4096, L_WQ + wo_ + kk * 4096 + 512);
#pragma unroll
        for (int kk = 0; kk < 2; ++kk) ah[kk] = trfrag(L_W1 + wo_ + kk * 4096, L_W1 + wo_ + kk * 4096 + 512);
        __builtin_amdgcn_sched_barrier(0);
        f32x4 Dq = {0.f, 0.f, 0.f, 0.f}, Dq2 = {0.f, 0.f, 0.f, 0.f}, Dh = {0.f, 0.f, 0.f, 0.f};
        Dq = mfma(aq[0], bq[0], Dq);
        Dq2 = mfma(aq[1], bq[1], Dq2);
        Dh = mfma(ah[0], bh[0], Dh);
        Dq = mfma(aq[2], bq[2], Dq);
        if (nq == 4) Dq2 = mfma(aq[3], bq[3], Dq2);
        Dh = mfma(ah[1], bh[1], Dh);
        Dq += Dq2;
        lds_signal(F_S1);
        float q[4], h[4];
#pragma unroll
        for (int r = 0; r < 4; ++r) { q[r] = fmaf(rstd, Dq[r], -mr * dpp<NBC4>(Dq[r])); h[r] = fmaf(rstd, Dh[r], -mr * dpp<NBC4>(Dh[r])); }
        if (n < 4) {
            stg(rs, qoff, cvtpk(q[0], q[1]), cvtpk(q[2], q[3]), T0, T1);
            if (hrole) stg(rs, hoff, cvtpk(h[0], h[1]), cvtpk(h[2], h[3]), T0, T1);
        }
    }
    STAMP(2);
    const int gid = b * 8 + kvh;
    v4u hx = v4u{0u, 0u, 0u, 0u};
    const bool hval = mlpo && lane < 28;
    {
        const int qo = WS_Q + (gid * 512 + (lane >> 5) * 128 + 32 * t + (lane & 31)) * 16;
        const int ho = hval ? WS_H + ((t * 7 + (lane >> 2)) * 448 + own * 4 + (lane & 3)) * 16 : qo;
        v4u x0, x1;
        wait_prog(nW + QPOLL);
#pragma unroll 1
        for (unsigned it = 0; it < SPIN_CAP; ++it) {
            cbar();
            NPOLL(9, 1);
            x0 = ldg(rs, qo); x1 = ldg(rs, qo + 256 * 16); hx = ldg(rs, ho);
            if (__all(tags_ok(x0, T0, T1) & tags_ok(x1, T0, T1) & (!hval | tags_ok(hx, T0, T1)))) break;
        }
        STAMP(3);
        const float q0 = x32_sum(bflo(x0.x) + bflo(x1.x)), q1 = x32_sum(bfhi(x0.x) + bfhi(x1.x));
        const float q2 = x32_sum(bflo(x0.z) + bflo(x1.z)), q3 = x32_sum(bfhi(x0.z) + bfhi(x1.z));
        if (lane < 32) *(LAS v2u*)(L_Q + (32 * t + lane) * 8) = v2u{cvtpk(q0, q1), cvtpk(q2, q3)};
        lds_signal(F_Q);
    }
    {
        const unsigned ka = L_K + t * 16896 + (n >> 1) * 1056 + (n & 1) * 512 + ((g ^ (n & 1)) * 16);
        wait_prog(nW + 4 * (t + 1));
        v4u kf[16];
#pragma unroll
        for (int i = 0; i < 16; ++i) kf[i] = lds128(ka + (i >> 3) * 8448 + (i & 7) * 64);
        spin_lds_ge(F_Q, 4);
        v4u qf[8];
#pragma unroll
        for (int ks = 0; ks < 8; ++ks) qf[ks] = lds128(L_Q + (n & 1) * 512 + ks * 64 + g * 16);
        __builtin_amdgcn_sched_barrier(0);
        f32x4 z0 = {0.f, 0.f, 0.f, 0.f}, z1 = {0.f, 0.f, 0.f, 0.f};
#pragma unroll
        for (int ks = 0; ks < 8; ++ks) { z0 = mfma(kf[ks], qf[ks], z0); z1 = mfma(kf[8 + ks], qf[ks], z1); }
        constexpr float C = 0.0625f * 1.4426950408889634f;
        const float z[4] = {(n & 2) ? z1[0] : z0[0], (n & 2) ? z1[1] : z0[1], (n & 2) ? z1[2] : z0[2], (n & 2) ? z1[3] : z0[3]};
        float m = fmaxf(fmaxf(z[0], z[1]), fmaxf(z[2], z[3])) * C;
        m = xrow_max(fmaxf(m, dpp<XOR2>(m)));
        float p[4];
#pragma unroll
        for (int r = 0; r < 4; ++r) p[r] = __builtin_amdgcn_exp2f(fmaf(z[r], C, -m));
        if (n < 4) *(LAS v2u*)(L_P + (n & 1) * 256 + (32 * t + 8 * g + 4 * (n >> 1)) * 2) = v2u{cvtpk(p[0], p[1]), cvtpk(p[2], p[3])};
        float l = (p[0] + p[1]) + (p[2] + p[3]);
        l = xrow_sum(l + dpp<XOR2>(l));
        if (lane < 2) { *(LAS float*)(L_MX + lane * 16 + t * 4) = m; *(LAS float*)(L_LS + lane * 16 + t * 4) = l; }
        lds_signal(F_P);
    }
    STAMP(4);
    if (mlpo) {
        const float hv[4] = {hval ? bflo(hx.x) : 0.f, hval ? bfhi(hx.x) : 0.f, hval ? bflo(hx.z) : 0.f, hval ? bfhi(hx.z) : 0.f};
        const float bb[4] = {bflo(b1v.x), bfhi(b1v.x), bflo(b1v.y), bfhi(b1v.y)};
        float act[4];
#pragma unroll
        for (int r = 0; r < 4; ++r) {
            float v = hv[r]; v += dpp<ROR8>(v); v += dpp<ROR4>(v); v = x16_sum(v) + bb[r];
            const float e = __builtin_amdgcn_exp2f(-1.4426950408889634f * fabsf(v));
            const float num = v > 0.f ? fmaf(2.f, e, 1.f) : e * (e + 2.f);
            const float den = v > 0.f ? fmaf(2.f * e, e, num) : num + 2.f;
            act[r] = v * num * __builtin_amdgcn_rcpf(den);
        }
        if (lane < 4) *(LAS v2u*)(L_AM + t * 32 + lane * 8) = v2u{cvtpk(act[0], act[1]), cvtpk(act[2], act[3])};
        lds_signal(F_AM);
    }
    {
        const unsigned e = r1 & 1;
        const unsigned vb = L_V + (2 * g + (r1 >> 1)) * 1088 + e * 512 + t * 128 + c4 * 8;
        const unsigned va0 = vb + e * 32, va1 = vb + (e ^ 1) * 32;
        const int ooff = WS_O + ((((kvh * 16 + (n & 1) * 8 + 2 * t + ((n >> 2) & 1)) * 4 + b) * 8 + s) * 8 + 4 * ((n >> 1) & 1) + g) * 16;
        const int soff = WS_S + ((((kvh * 2 + (lane & 1)) * 4 + b) * 8 + s) * 4 + ((lane >> 1) & 3)) * 16;
        spin_lds_ge(F_P, 4);
        v4u pf[4];
#pragma unroll
        for (int kk = 0; kk < 4; ++kk) pf[kk] = lds128(L_P + (n & 1) * 256 + kk * 64 + g * 16);
        const v4u mx = lds128(L_MX + (n & 1) * 16), ls = lds128(L_LS + (n & 1) * 16);
        const float mk[4] = {__uint_as_float(mx.x), __uint_as_float(mx.y), __uint_as_float(mx.z), __uint_as_float(mx.w)};
        const float mW = fmaxf(fmaxf(mk[0], mk[1]), fmaxf(mk[2], mk[3]));
        float fk[4];
#pragma unroll
        for (int kk = 0; kk < 4; ++kk) fk[kk] = __builtin_amdgcn_exp2f(mk[kk] - mW);
        const float lW = fmaf(fk[0], __uint_as_float(ls.x), fk[1] * __uint_as_float(ls.y)) + fmaf(fk[2], __uint_as_float(ls.z), fk[3] * __uint_as_float(ls.w));
        if (t == 0 && lane < 8) stg(rs, soff, __float_as_uint(mW), __float_as_uint(lW), T0, T1);
        f32x4 o[4];
        unsigned seen = 0;
        v4u vf[2][4];
        wait_prog_c(nW + 20, seen);
#pragma unroll
        for (int aa = 0; aa < 4; ++aa) { const unsigned ad = ((aa & 1) ? va1 : va0) + (aa >> 1) * 64; vf[0][aa] = trfrag(ad, ad + 8704); }
#pragma unroll
        for (int kk = 0; kk < 4; ++kk) {
            if (kk < 3) {
                wait_prog_c(nW + 16 + 4 * (kk + 2), seen);
#pragma unroll
                for (int aa = 0; aa < 4; ++aa) { const unsigned ad = ((aa & 1) ? va1 : va0) + (aa >> 1) * 64 + (kk + 1) * 17408; vf[(kk + 1) & 1][aa] = trfrag(ad, ad + 8704); }
            }
#pragma unroll
            for (int aa = 0; aa < 4; ++aa) { const f32x4 d = mfma(vf[kk & 1][aa], pf[kk], f32x4{0.f, 0.f, 0.f, 0.f}); o[aa] = kk == 0 ? fk[0] * d : o[aa] + fk[kk] * d; }
        }
        float v[4];
#pragma unroll
        for (int r = 0; r < 4; ++r) { const float lo = (n & 2) ? o[1][r] : o[0][r], hi = (n & 2) ? o[3][r] : o[2][r]; v[r] = (n & 4) ? hi : lo; }
        if (n < 8) stg(rs, ooff, cvtpk(v[0], v[1]), cvtpk(v[2], v[3]), T0, T1);
    }
    STAMP(5);
    if (owner) {
        const unsigned ba = L_K + (4 * g + r1) * 928 + t * 224 + c4 * 8;
        f32x4 acc[7];
        v4u wf[7];
        wait_prog(nW + 32 + 4);
        if (mlpo) {
            wait_lds_ge(F_AM, 4);
            const v2u am = lds64(L_AM + (n & 3) * 32 + g * 8);
            const v4u B = v4u{am.x, am.y, 0u, 0u};
#pragma unroll
            for (int T = 0; T < 7; ++T) { const v2u wl = tr64(ba + T * 32); acc[T] = mfma(v4u{wl.x, wl.y, 0u, 0u}, B, f32x4{0.f, 0.f, 0.f, 0.f}); }
        } else {
#pragma unroll
            for (int T = 0; T < 7; ++T) acc[T] = f32x4{0.f, 0.f, 0.f, 0.f};
        }
        const int h = j >> 3;
        const int oo = WS_O + ((own * 4 + t) * 64 + lane) * 16, so = WS_S + (((kvh * 2 + h) * 4 + t) * 8 + (lane >> 3)) * 64;
        const int foff0 = WS_FIN + ((((n & 3) * 28 + 7 * t + (n >> 2)) * 128 + own) * 4 + g) * 16;
        wait_prog(nW + 32 + 12);
#pragma unroll
        for (int T = 0; T < 7; ++T) wf[T] = trfrag(ba + 16 * 928 + T * 32, ba + 32 * 928 + T * 32);
        v4u og, sg;
#pragma unroll 1
        for (unsigned it = 0; it < SPIN_CAP; ++it) {
            cbar();
            og = ldg(rs, oo); sg = ldg(rs, so);
            if (__all(tags_ok(og, T0, T1) & tags_ok(sg, T0, T1))) break;
        }
        STAMP(6);
        const float m = __uint_as_float(sg.x), l = __uint_as_float(sg.z);
        const float M = xrow_max(fmaxf(m, dpp<ROR8>(m))), e = __builtin_amdgcn_exp2f(m - M);
        float le = l * e; le += dpp<ROR8>(le);
        const float wgt = e * __builtin_amdgcn_rcpf(xrow_sum(le));
        float v[4] = {bflo(og.x) * wgt, bfhi(og.x) * wgt, bflo(og.z) * wgt, bfhi(og.z) * wgt};
#pragma unroll
        for (int r = 0; r < 4; ++r) { v[r] += dpp<ROR8>(v[r]); v[r] = xrow_sum(v[r]); }
        if (lane < 8) *(LAS v2u*)(L_A + t * 64 + (8 * (lane & 3) + 4 * (lane >> 2)) * 2) = v2u{cvtpk(v[0], v[1]), cvtpk(v[2], v[3])};
        lds_signal(F_A);
        spin_lds_ge(F_A, 4);
        const v4u B = lds128(L_A + (n & 3) * 64 + g * 16);
#pragma unroll
        for (int T = 0; T < 7; ++T) acc[T] = mfma(wf[T], B, acc[T]);
        unsigned p0[7], p1[7];
#pragma unroll
        for (int T = 0; T < 7; ++T) { p0[T] = cvtpk(acc[T][0], acc[T][1]); p1[T] = cvtpk(acc[T][2], acc[T][3]); }
        const int q4 = n >> 2;
        const unsigned a0 = q4 == 0 ? p0[0] : q4 == 1 ? p0[1] : q4 == 2 ? p0[2] : p0[3], a1 = q4 == 0 ? p1[0] : q4 == 1 ? p1[1] : q4 == 2 ? p1[2] : p1[3];
        const unsigned c0 = q4 == 0 ? p0[4] : q4 == 1 ? p0[5] : p0[6], c1 = q4 == 0 ? p1[4] : q4 == 1 ? p1[5] : p1[6];
        stg(rs, foff0, a0, a1, T0, T1);
        if (q4 < 3) stg(rs, foff0 + 4 * 128 * 4 * 16, c0, c1, T0, T1);
    }
    STAMP(7);
    if (t == 3 && fin) {
        const int fo = WS_FIN + ((fb * 28 + fc) * 512 + lane) * 16;
        {
#pragma unroll 1
            for (unsigned it = 0; it < SPIN_CAP; ++it) {
                cbar();
                bool ok = false;
                if (lane < 2) ok = tags_ok(ldg(rs, WS_FIN + ((fb * 28 + fc) * 512 + (kvh * 16 + lane * 8) * 4) * 16), T0, T1);
                if (__any(ok)) break;
                __builtin_amdgcn_s_sleep(FPROBE);
            }
        }
        v4u f[8];
#pragma unroll 1
        for (unsigned it = 0; it < SPIN_CAP; ++it) {
            cbar();
            bool ok = true;
#pragma unroll
            for (int i = 0; i < 8; ++i) f[i] = ldg(rs, fo + i * 1024);
#pragma unroll
            for (int i = 0; i < 8; ++i) ok &= tags_ok(f[i], T0, T1);
            if (__all(ok)) break;
        }
        const v4u sel = v4u{n == 0 ? 0x00003F80u : n == 1 ? 0x3F800000u : 0u, 0u, n == 2 ? 0x00003F80u : n == 3 ? 0x3F800000u : 0u, 0u};
        f32x4 d0 = {0.f, 0.f, 0.f, 0.f}, d1 = {0.f, 0.f, 0.f, 0.f};
#pragma unroll
        for (int i = 0; i < 8; i += 2) { d0 = mfma(sel, f[i], d0); d1 = mfma(sel, f[i + 1], d1); }
        float v[4];
#pragma unroll
        for (int r = 0; r < 4; ++r) { float x = d0[r] + d1[r]; x += dpp<ROR8>(x); v[r] = x + dpp<ROR4>(x); }
        if (lane < 4) {
            asm volatile("" : "+v"(xres));
            *(GAS v2u*)(a.out + fb * 448 + 16 * fc + 4 * lane) = v2u{cvtpk(bflo(xres.x) + v[0], bfhi(xres.x) + v[1]), cvtpk(bflo(xres.y) + v[2], bfhi(xres.y) + v[3])};
        }
        if (blk == 128 && lane == 0) *(GAS unsigned*)(a.ws + WS_E) = Eu + 1u;
        STAMP(8);
    }
    STAMP_FLUSH();
}
}

extern "C" void kernel_launch(void* const* d_in, const int* in_sizes, int n_in, void* d_out, int out_size, void* d_ws, size_t ws_size, hipStream_t stream) {
    (void)in_sizes; (void)n_in; (void)out_size; (void)ws_size;
    fk::Args a;
    a.x = (const unsigned char*)d_in[0]; a.k = (const unsigned char*)d_in[1]; a.v = (const unsigned char*)d_in[2];
    a.wq = (const unsigned char*)d_in[3]; a.wo = (const unsigned char*)d_in[4]; a.w1 = (const unsigned char*)d_in[6];
    a.b1 = (const unsigned char*)d_in[7]; a.w2 = (const unsigned char*)d_in[8]; a.out = (unsigned short*)d_out; a.ws = (unsigned char*)d_ws;
    (void)hipFuncSetAttribute((const void*)fk::fused, hipFuncAttributeMaxDynamicSharedMemorySize, fk::LDS_BYTES);
    hipLaunchKernelGGL(fk::fused, dim3(256), dim3(512), fk::LDS_BYTES, stream, a);
}
```

```cpp
#include <hip/hip_runtime.h>
#include <hip/hip_bf16.h>
#include <stdint.h>

#define GAS __attribute__((address_space(1)))
#define LAS __attribute__((address_space(3)))
typedef unsigned v4u __attribute__((ext_vector_type(4)));
typedef unsigned v2u __attribute__((ext_vector_type(2)));
typedef float f32x4 __attribute__((ext_vector_type(4)));
typedef float f32x2 __attribute__((ext_vector_type(2)));
typedef short s16x4 __attribute__((ext_vector_type(4)));
typedef __bf16 bf16x8_t __attribute__((ext_vector_type(8)));
typedef __bf16 bf16x2_t __attribute__((ext_vector_type(2)));

#define STAMPS 0
#define SETPRIO 3
#define QPOLL 12
#define FPROBE 6
#define WINK 6
#define WINSW 4
#define BS 4
#define WINL 8
#define PREFETCH_CODE 2

namespace fk {
constexpr int AUX_SC1 = 16;
constexpr int L_K = 0;
constexpr int L_WQ = 67584;
constexpr int L_V = L_WQ + 16384;
constexpr int L_X = L_V + 69632 - 8192 - 4608;
constexpr int L_W1 = L_V + 69632 - 8192;
constexpr int L_M = L_V + 69632;
constexpr int L_Q = L_M;
constexpr int L_P = L_Q + 1024;
constexpr int L_A = L_P + 512;
constexpr int L_AM = L_A + 256;
constexpr int L_MX = L_AM + 128;
constexpr int L_LS = L_MX + 32;
constexpr int L_F = L_LS + 32;
constexpr int F_S1 = L_F + 16, F_QK = L_F + 20, F_M = L_F + 24, F_P = L_F + 28, F_Q = L_F + 32, F_A = L_F + 36, F_AM = L_F + 40;
constexpr int L_PF = L_F + 64;
constexpr int LDS_BYTES = L_PF + 1024;
static_assert(L_X >= L_V + 52 * 1088, "X / W1 images alias V instructions >= 52 only");
static_assert(48 * 928 <= 67584, "projection rows inside the K image");
static_assert(LDS_BYTES <= 163840, "LDS");
static_assert((L_F & 15) == 0 && (L_MX & 15) == 0 && (L_A & 15) == 0, "alignment");
constexpr int WS_E = 0;
constexpr int WS_Q = 4096;
constexpr int WS_H = WS_Q + 4 * 8 * 4 * 128 * 16;
constexpr int WS_O = WS_H + 4 * 7 * 448 * 16;
constexpr int WS_S = WS_O + 128 * 256 * 16;
constexpr int WS_FIN = WS_S + 512 * 64;
constexpr int WS_ST = WS_FIN + 4 * 28 * 512 * 16;
constexpr int WS_END = WS_ST + 256 * 8 * 16 * 8;
constexpr unsigned SPIN_CAP = 1u << 17;

struct Args {
    const unsigned char* x; const unsigned char* k; const unsigned char* v; const unsigned char* wq; const unsigned char* wo;
    const unsigned char* w1; const unsigned char* b1; const unsigned char* w2; unsigned short* out; unsigned char* ws;
};

__device__ __forceinline__ unsigned long long rt() { return __builtin_amdgcn_s_memrealtime(); }
template <bool NT> __device__ __forceinline__ void dma16(unsigned voff, unsigned long long sbase, unsigned lds) {
    if (NT) asm volatile("s_mov_b32 m0, %2\n\ts_nop 0\n\tglobal_load_lds_dwordx4 %0, %1 nt" :: "v"(voff), "s"(sbase), "s"(lds) : "memory");
    else    asm volatile("s_mov_b32 m0, %2\n\ts_nop 0\n\tglobal_load_lds_dwordx4 %0, %1" :: "v"(voff), "s"(sbase), "s"(lds) : "memory");
}
template <int N> __device__ __forceinline__ void waitv() { asm volatile("s_waitcnt vmcnt(%0)" :: "i"(N) : "memory"); }
__device__ __forceinline__ void cbar() { asm volatile("" ::: "memory"); }

__device__ __forceinline__ f32x4 mfma(v4u a, v4u b, f32x4 c) {
    return __builtin_amdgcn_mfma_f32_16x16x32_bf16(__builtin_bit_cast(bf16x8_t, a), __builtin_bit_cast(bf16x8_t, b), c, 0, 0, 0);
}
__device__ __forceinline__ v4u lds128(unsigned a) { return *(const LAS v4u*)a; }
__device__ __forceinline__ v2u lds64(unsigned a) { return *(const LAS v2u*)a; }
__device__ __forceinline__ v2u tr64(unsigned a) { return __builtin_bit_cast(v2u, __builtin_amdgcn_ds_read_tr16_b64_v4i16((LAS s16x4*)a)); }
__device__ __forceinline__ v4u trfrag(unsigned a0, unsigned a1) { v2u lo = tr64(a0), hi = tr64(a1); return v4u{lo.x, lo.y, hi.x, hi.y}; }
__device__ __forceinline__ unsigned cvtpk(float lo, float hi) { return __builtin_bit_cast(unsigned, __builtin_convertvector(f32x2{lo, hi}, bf16x2_t)); }
__device__ __forceinline__ float bflo(unsigned u) { return __builtin_bit_cast(float, u << 16); }
__device__ __forceinline__ float bfhi(unsigned u) { return __builtin_bit_cast(float, u & 0xffff0000u); }
template <int CTRL> __device__ __forceinline__ float dpp(float x) {
    return __builtin_bit_cast(float, __builtin_amdgcn_mov_dpp(__builtin_bit_cast(int, x), CTRL, 0xf, 0xf, true));
}
constexpr int XOR1 = 0xB1, XOR2 = 0x4E, HMIR = 0x141, ROR4 = 0x124, ROR8 = 0x128, NBC4 = 0x154;
__device__ __forceinline__ float xrow_max(float x) {
    auto s = __builtin_amdgcn_permlane16_swap(__float_as_uint(x), __float_as_uint(x), false, false);
    x = fmaxf(__uint_as_float(s[0]), __uint_as_float(s[1]));
    auto t = __builtin_amdgcn_permlane32_swap(__float_as_uint(x), __float_as_uint(x), false, false);
    return fmaxf(__uint_as_float(t[0]), __uint_as_float(t[1]));
}
__device__ __forceinline__ float x32_sum(float x) {
    auto t = __builtin_amdgcn_permlane32_swap(__float_as_uint(x), __float_as_uint(x), false, false);
    return __uint_as_float(t[0]) + __uint_as_float(t[1]);
}
__device__ __forceinline__ float x16_sum(float x) {
    auto s = __builtin_amdgcn_permlane16_swap(__float_as_uint(x), __float_as_uint(x), false, false);
    return __uint_as_float(s[0]) + __uint_as_float(s[1]);
}
__device__ __forceinline__ float xrow_sum(float x) {
    auto s = __builtin_amdgcn_permlane16_swap(__float_as_uint(x), __float_as_uint(x), false, false);
    return x32_sum(__uint_as_float(s[0]) + __uint_as_float(s[1]));
}
__device__ __forceinline__ float par16_sum(float x) { x += dpp<XOR2>(x); x += dpp<ROR4>(x); return x + dpp<ROR8>(x); }
__device__ __forceinline__ float bperm(int srclane, float v) { return __builtin_bit_cast(float, __builtin_amdgcn_ds_bpermute(srclane * 4, __builtin_bit_cast(int, v))); }

__device__ __forceinline__ void wait_lds_ge(unsigned addr, unsigned want) {
#pragma unroll 1
    for (unsigned i = 0; i < (1u << 22); ++i) { if ((unsigned)__builtin_amdgcn_readfirstlane(*(volatile LAS unsigned*)addr) >= want) break; __builtin_amdgcn_s_sleep(1); }
    cbar();
}
__device__ __forceinline__ void spin_lds_ge(unsigned addr, unsigned want) {
#pragma unroll 1
    for (unsigned i = 0; i < (1u << 24); ++i) { if ((unsigned)__builtin_amdgcn_readfirstlane(*(volatile LAS unsigned*)addr) >= want) break; }
    cbar();
}
__device__ __forceinline__ void wait_prog(unsigned want) {
#pragma unroll 1
    for (unsigned i = 0; i < (1u << 22); ++i) {
        const v4u p = *(volatile LAS v4u*)L_F;
        const unsigned m01 = p.x < p.y ? p.x : p.y, m23 = p.z < p.w ? p.z : p.w;
        if ((unsigned)__builtin_amdgcn_readfirstlane(m01 < m23 ? m01 : m23) >= want) break;
        __builtin_amdgcn_s_sleep(1);
    }
    cbar();
}
__device__ __forceinline__ void wait_prog_c(unsigned want, unsigned& seen) {
#pragma unroll 1
    for (unsigned i = 0; i < (1u << 24) && seen < want; ++i) {
        const v4u p = *(volatile LAS v4u*)L_F;
        const unsigned m01 = p.x < p.y ? p.x : p.y, m23 = p.z < p.w ? p.z : p.w;
        seen = (unsigned)__builtin_amdgcn_readfirstlane(m01 < m23 ? m01 : m23);
    }
    cbar();
}
__device__ __forceinline__ void lds_signal(unsigned addr) {
    asm volatile("s_waitcnt lgkmcnt(0)\n\ts_mov_b64 exec, 1\n\tds_add_u32 %0, %1\n\ts_mov_b64 exec, -1" :: "v"(addr), "v"(1u) : "memory");
}
__device__ __forceinline__ bool tags_ok(v4u g, unsigned t0, unsigned t1) { return (g.y == t0) & (g.w == t1); }
__device__ __forceinline__ v4u ldg(__amdgpu_buffer_rsrc_t rs, int off) { return __builtin_amdgcn_raw_buffer_load_b128(rs, off, 0, AUX_SC1); }
__device__ __forceinline__ void stg(__amdgpu_buffer_rsrc_t rs, int off, unsigned p0, unsigned p1, unsigned t0, unsigned t1) { __builtin_amdgcn_raw_buffer_store_b128(v4u{p0, t0, p1, t1}, rs, off, 0, AUX_SC1); }

#define STAMP(i) do { } while (0)
#define NPOLL(i, v) do { } while (0)
#define STAMP_DECL do { } while (0)
#define STAMP_FLUSH() do { } while (0)

__global__ void __launch_bounds__(512, 2) fused(Args a) {
    const int tid = threadIdx.x, lane = tid & 63, w = __builtin_amdgcn_readfirstlane(tid >> 6);
    const int blk = blockIdx.x, kvh = blk & 7, j = blk >> 3, b = j >> 3, s = j & 7;
    const bool hrole = blk < 196, owner = j < 16;
    const int own = kvh * 16 + j;
    const bool mlpo = owner && own < 112;
    const int nq = b < 2 ? 4 : 3;
    const unsigned nW = 1 + nq + (hrole ? 2 : 0);
    STAMP_DECL;
    STAMP(0);
    {
        unsigned long long pc;
        asm volatile("s_getpc_b64 %0" : "=s"(pc));
        pc = (pc & ~0xFFull) + w * 1536;
        dma16<false>(lane * 16, pc, L_PF);
        dma16<false>(lane * 16, pc + 1024, L_PF);
    }
    if (__builtin_expect(w < 4, 1)) {
        const int L = w;
        if (lane < 56) dma16<false>(lane * 16, (unsigned long long)a.x + L * 896, L_X + L * 912);
        const unsigned f = ((lane >> 4) & 1) | ((L & 1) << 1), sp = lane & 7, gp = (((sp >> 1) ^ f) << 1) | (sp & 1);
        {
            const int r0 = b < 2 ? b * 128 : 256 + (b - 2) * 96;
            unsigned long long p = (unsigned long long)a.wq + (size_t)(r0 + 8 * L) * 8192 + (kvh * 512 + s * 64) * 2; unsigned l = L_WQ + L * 1024;
            const unsigned voff = (lane >> 3) * 8192 + gp * 16;
#pragma unroll 1
            for (int i = 0; i < nq; ++i) { dma16<true>(voff, p, l); p += 32 * 8192; l += 4096; }
        }
        if (hrole) {
            const int cb = blk % 28, rb = blk / 28;
            unsigned long long p = (unsigned long long)a.w1 + (size_t)(rb * 64 + 8 * L) * 3584 + cb * 128;
            const unsigned voff = (lane >> 3) * 3584 + gp * 16;
            dma16<true>(voff, p, L_W1 + L * 1024);
            dma16<true>(voff, p + 32 * 3584, L_W1 + L * 1024 + 4096);
        }
        const size_t kvbase = ((size_t)(b * 8 + kvh) * 4096 + 3072) * 512 + (size_t)s * (BS * 1024) + L * 1024;
#define KVSTEP(i) ((((i) + 1) * 4 % BS == 0) ? (7 * BS + 4) * 1024 : 4096)
        unsigned n = nW;
        volatile LAS unsigned* prog = (volatile LAS unsigned*)(L_F + 4 * L);
        {
            const unsigned voff = (lane >> 5) * 512 + ((lane & 31) ^ (lane >> 5)) * 16;
            unsigned long long p = (unsigned long long)a.k + kvbase; unsigned l = L_K + L * 1056;
            dma16<true>(voff, p, l); ++n; p += KVSTEP(0); l += 4224;
            __builtin_amdgcn_s_barrier();
#pragma unroll 1
            for (int i = 1; i < 16; ++i) { dma16<true>(voff, p, l); ++n; waitv<WINK - 1>(); const int d = (int)n - (WINK - 1); *prog = d > 0 ? d : 0; p += KVSTEP(i); l += 4224; }
        }
        STAMP(1);
        {
            const unsigned voff = (lane >> 5) * 512 + ((lane & 31) ^ ((lane >> 5) << 1)) * 16;
            unsigned long long p = (unsigned long long)a.v + kvbase; unsigned l = L_V + L * 1088;
#pragma unroll 1
            for (int i = 0; i < 16; ++i) {
                if (i == 12) wait_lds_ge(F_S1, 4);
                dma16<true>(voff, p, l); ++n;
                if (i < WINSW) { waitv<WINK - 1>(); *prog = n - (WINK - 1); } else { waitv<WINL - 1>(); *prog = n - (WINL - 1); }
                p += KVSTEP(i); l += 4352;
            }
        }
        STAMP(2);
        if (owner) {
            wait_lds_ge(F_P, 4);
            const unsigned voff = lane * 16;
            unsigned l = L_K + L * 928;
            const unsigned long long w2p = (unsigned long long)a.w2 + (unsigned)(((mlpo ? own : 0) * 16 + L) * 896);
            const unsigned long long wop = (unsigned long long)a.wo + (unsigned)((own * 32 + L) * 896);
            if (lane < 56) {
#pragma unroll
                for (int i = 0; i < 4; ++i) dma16<true>(voff, w2p + i * 4 * 896, l + i * 3712);
            }
            waitv<8>(); *prog = n - 4;
            waitv<4>(); *prog = n;
            STAMP(4);
            if (lane < 56) {
#pragma unroll
                for (int i = 0; i < 8; ++i) dma16<true>(voff, wop + i * 4 * 896, l + (4 + i) * 3712);
            }
            waitv<8>(); *prog = n + 4;
            waitv<0>(); *prog = n + 12;
        } else {
            waitv<4>(); *prog = n - 4;
            waitv<0>(); *prog = n;
            STAMP(4);
        }
        STAMP(3);
        STAMP_FLUSH();
        return;
    }
    const int t = w - 4, n = lane & 15, g = lane >> 4;
    __builtin_amdgcn_s_setprio(SETPRIO);
    if (t == 0) {
        if (lane < 16) *(LAS unsigned*)(L_F + 4 * lane) = 0u;
        if (lane < 56) *(LAS v4u*)(L_X + 4 * 912 + lane * 16) = v4u{0x3F803F80u, 0x3F803F80u, 0x3F803F80u, 0x3F803F80u};
    }
    asm volatile("s_waitcnt lgkmcnt(0)" ::: "memory");
    __builtin_amdgcn_s_barrier();
    cbar();
    const __amdgpu_buffer_rsrc_t rs = __builtin_amdgcn_make_buffer_rsrc(a.ws, 0, WS_END, 0x00020000);
    const unsigned Eu = *(const __attribute__((address_space(4))) unsigned*)(a.ws + WS_E);
    const unsigned T0 = (Eu * 0x9E3779B1u + 0x7F4A7C15u) & 0xBFFFBFFFu, T1 = ((Eu ^ 0x5BD1E995u) * 0x85EBCA6Bu + 0x165667B1u) & 0xBFFFBFFFu;
    v2u b1v = v2u{0u, 0u};
    if (mlpo && lane < 32) b1v = *(const GAS v2u*)(a.b1 + (own * 16 + (lane & 3) * 4) * 2);
    const int fi = blk - 128, fb = fi / 28, fc = fi % 28;
    const bool fin = fi >= 0 && fi < 112;
    v2u xres = v2u{0u, 0u};
    if (t == 3 && fin && lane < 4) xres = *(const GAS v2u*)(a.x + (fb * 448 + 16 * fc + 4 * lane) * 2);
    const unsigned r1 = n >> 2, c4 = n & 3;

    {
        const unsigned xa = L_X + (n < 4 ? n : 4) * 912 + g * 16;
        const unsigned fs = ((r1 >> 1) & 1) | ((g & 1) << 1);
        const unsigned wo_ = (8 * g + r1) * 128 + ((t ^ fs) * 32) + c4 * 8;
        const int ks0 = b < 2 ? b * 4 : 8 + (b - 2) * 3, rb = blk / 28;
        const int qoff = WS_Q + (((n * 8 + kvh) * 4 + b) * 128 + s * 16 + t * 4 + g) * 16;
        const int hoff = WS_H + ((n * 7 + rb) * 448 + (blk % 28) * 16 + t * 4 + g) * 16;
        wait_prog(1);
        v4u xf[14];
#pragma unroll
        for (int ks = 0; ks < 14; ++ks) xf[ks] = lds128(xa + 64 * ks);
        v4u bq[4], bh[2];
#pragma unroll
        for (int kk = 0; kk < 4; ++kk) bq[kk] = lds128(xa + 64 * (ks0 + (kk < nq ? kk : 0)));
#pragma unroll
        for (int kk = 0; kk < 2; ++kk) bh[kk] = lds128(xa + 64 * ((hrole ? 2 * rb : 0) + kk));
        __builtin_amdgcn_sched_barrier(0);
        f32x4 G0 = {0.f, 0.f, 0.f, 0.f}, G1 = {0.f, 0.f, 0.f, 0.f};
#pragma unroll
        for (int ks = 0; ks < 14; ks += 2) { G0 = mfma(xf[ks], xf[ks], G0); G1 = mfma(xf[ks + 1], xf[ks + 1], G1); }
        const f32x4 G = G0 + G1;
        const float gsel = n == 0 ? G[0] : n == 1 ? G[1] : n == 2 ? G[2] : G[3];
        const float sq = bperm(n & 3, gsel), sm = bperm(16 + (n & 3), G[0]);
        const float mean = sm * (1.0f / 448.0f);
        const float var = fmaxf(fmaf(sq, 1.0f / 448.0f, -mean * mean), 0.f);
        const float rstd = __builtin_amdgcn_rsqf(var + 1e-5f), mr = mean * rstd;
        asm volatile("" :: "v"(rstd), "v"(mr), "v"(qoff), "v"(hoff));
        wait_prog(nW);
        STAMP(1);
        v4u aq[4], ah[2];
#pragma unroll
        for (int kk = 0; kk < 4; ++kk) aq[kk] = trfrag(L_WQ + wo_ + kk * 4096, L_WQ + wo_ + kk * 4096 + 512);
#pragma unroll
        for (int kk = 0; kk < 2; ++kk) ah[kk] = trfrag(L_W1 + wo_ + kk * 4096, L_W1 + wo_ + kk * 4096 + 512);
        __builtin_amdgcn_sched_barrier(0);
        f32x4 Dq = {0.f, 0.f, 0.f, 0.f}, Dq2 = {0.f, 0.f, 0.f, 0.f}, Dh = {0.f, 0.f, 0.f, 0.f};
        Dq = mfma(aq[0], bq[0], Dq);
        Dq2 = mfma(aq[1], bq[1], Dq2);
        Dh = mfma(ah[0], bh[0], Dh);
        Dq = mfma(aq[2], bq[2], Dq);
        if (nq == 4) Dq2 = mfma(aq[3], bq[3], Dq2);
        Dh = mfma(ah[1], bh[1], Dh);
        Dq += Dq2;
        lds_signal(F_S1);
        float q[4], h[4];
#pragma unroll
        for (int r = 0; r < 4; ++r) { q[r] = fmaf(rstd, Dq[r], -mr * dpp<NBC4>(Dq[r])); h[r] = fmaf(rstd, Dh[r], -mr * dpp<NBC4>(Dh[r])); }
        if (n < 4) {
            stg(rs, qoff, cvtpk(q[0], q[1]), cvtpk(q[2], q[3]), T0, T1);
            if (hrole) stg(rs, hoff, cvtpk(h[0], h[1]), cvtpk(h[2], h[3]), T0, T1);
        }
    }
    STAMP(2);
    const int gid = b * 8 + kvh;
    v4u hx = v4u{0u, 0u, 0u, 0u};
    const bool hval = mlpo && lane < 28;
    {
        const int qo = WS_Q + (gid * 512 + (lane >> 5) * 128 + 32 * t + (lane & 31)) * 16;
        const int ho = hval ? WS_H + ((t * 7 + (lane >> 2)) * 448 + own * 4 + (lane & 3)) * 16 : qo;
        v4u x0, x1;
        wait_prog(nW + QPOLL);
#pragma unroll 1
        for (unsigned it = 0; it < SPIN_CAP; ++it) {
            cbar();
            NPOLL(9, 1);
            x0 = ldg(rs, qo); x1 = ldg(rs, qo + 256 * 16); hx = ldg(rs, ho);
            if (__all(tags_ok(x0, T0, T1) & tags_ok(x1, T0, T1) & (!hval | tags_ok(hx, T0, T1)))) break;
        }
        STAMP(3);
        const float q0 = x32_sum(bflo(x0.x) + bflo(x1.x)), q1 = x32_sum(bfhi(x0.x) + bfhi(x1.x));
        const float q2 = x32_sum(bflo(x0.z) + bflo(x1.z)), q3 = x32_sum(bfhi(x0.z) + bfhi(x1.z));
        if (lane < 32) *(LAS v2u*)(L_Q + (32 * t + lane) * 8) = v2u{cvtpk(q0, q1), cvtpk(q2, q3)};
        lds_signal(F_Q);
    }
    {
        const unsigned ka = L_K + t * 16896 + (n >> 1) * 1056 + (n & 1) * 512 + ((g ^ (n & 1)) * 16);
        wait_prog(nW + 4 * (t + 1));
        v4u kf[16];
#pragma unroll
        for (int i = 0; i < 16; ++i) kf[i] = lds128(ka + (i >> 3) * 8448 + (i & 7) * 64);
        spin_lds_ge(F_Q, 4);
        v4u qf[8];
#pragma unroll
        for (int ks = 0; ks < 8; ++ks) qf[ks] = lds128(L_Q + (n & 1) * 512 + ks * 64 + g * 16);
        __builtin_amdgcn_sched_barrier(0);
        f32x4 z0 = {0.f, 0.f, 0.f, 0.f}, z1 = {0.f, 0.f, 0.f, 0.f};
#pragma unroll
        for (int ks = 0; ks < 8; ++ks) { z0 = mfma(kf[ks], qf[ks], z0); z1 = mfma(kf[8 + ks], qf[ks], z1); }
        constexpr float C = 0.0625f * 1.4426950408889634f;
        const float z[4] = {(n & 2) ? z1[0] : z0[0], (n & 2) ? z1[1] : z0[1], (n & 2) ? z1[2] : z0[2], (n & 2) ? z1[3] : z0[3]};
        float m = fmaxf(fmaxf(z[0], z[1]), fmaxf(z[2], z[3])) * C;
        m = xrow_max(fmaxf(m, dpp<XOR2>(m)));
        float p[4];
#pragma unroll
        for (int r = 0; r < 4; ++r) p[r] = __builtin_amdgcn_exp2f(fmaf(z[r], C, -m));
        if (n < 4) *(LAS v2u*)(L_P + (n & 1) * 256 + (32 * t + 8 * g + 4 * (n >> 1)) * 2) = v2u{cvtpk(p[0], p[1]), cvtpk(p[2], p[3])};
        float l = (p[0] + p[1]) + (p[2] + p[3]);
        l = xrow_sum(l + dpp<XOR2>(l));
        if (lane < 2) { *(LAS float*)(L_MX + lane * 16 + t * 4) = m; *(LAS float*)(L_LS + lane * 16 + t * 4) = l; }
        lds_signal(F_P);
    }
    STAMP(4);
    if (mlpo) {
        const float hv[4] = {hval ? bflo(hx.x) : 0.f, hval ? bfhi(hx.x) : 0.f, hval ? bflo(hx.z) : 0.f, hval ? bfhi(hx.z) : 0.f};
        const float bb[4] = {bflo(b1v.x), bfhi(b1v.x), bflo(b1v.y), bfhi(b1v.y)};
        float act[4];
#pragma unroll
        for (int r = 0; r < 4; ++r) {
            float v = hv[r]; v += dpp<ROR8>(v); v += dpp<ROR4>(v); v = x16_sum(v) + bb[r];
            const float e = __builtin_amdgcn_exp2f(-1.4426950408889634f * fabsf(v));
            const float num = v > 0.f ? fmaf(2.f, e, 1.f) : e * (e + 2.f);
            const float den = v > 0.f ? fmaf(2.f * e, e, num) : num + 2.f;
            act[r] = v * num * __builtin_amdgcn_rcpf(den);
        }
        if (lane < 4) *(LAS v2u*)(L_AM + t * 32 + lane * 8) = v2u{cvtpk(act[0], act[1]), cvtpk(act[2], act[3])};
        lds_signal(F_AM);
    }
    {
        const unsigned e = r1 & 1;
        const unsigned vb = L_V + (2 * g + (r1 >> 1)) * 1088 + e * 512 + t * 128 + c4 * 8;
        const unsigned va0 = vb + e * 32, va1 = vb + (e ^ 1) * 32;
        const int ooff = WS_O + ((((kvh * 16 + (n & 1) * 8 + 2 * t + ((n >> 2) & 1)) * 4 + b) * 8 + s) * 8 + 4 * ((n >> 1) & 1) + g) * 16;
        const int soff = WS_S + ((((kvh * 2 + (lane & 1)) * 4 + b) * 8 + s) * 4 + ((lane >> 1) & 3)) * 16;
        spin_lds_ge(F_P, 4);
        v4u pf[4];
#pragma unroll
        for (int kk = 0; kk < 4; ++kk) pf[kk] = lds128(L_P + (n & 1) * 256 + kk * 64 + g * 16);
        const v4u mx = lds128(L_MX + (n & 1) * 16), ls = lds128(L_LS + (n & 1) * 16);
        const float mk[4] = {__uint_as_float(mx.x), __uint_as_float(mx.y), __uint_as_float(mx.z), __uint_as_float(mx.w)};
        const float mW = fmaxf(fmaxf(mk[0], mk[1]), fmaxf(mk[2], mk[3]));
        float fk[4];
#pragma unroll
        for (int kk = 0; kk < 4; ++kk) fk[kk] = __builtin_amdgcn_exp2f(mk[kk] - mW);
        const float lW = fmaf(fk[0], __uint_as_float(ls.x), fk[1] * __uint_as_float(ls.y)) + fmaf(fk[2], __uint_as_float(ls.z), fk[3] * __uint_as_float(ls.w));
        if (t == 0 && lane < 8) stg(rs, soff, __float_as_uint(mW), __float_as_uint(lW), T0, T1);
        f32x4 o[4];
        unsigned seen = 0;
        v4u vf[2][4];
        wait_prog_c(nW + 20, seen);
#pragma unroll
        for (int aa = 0; aa < 4; ++aa) { const unsigned ad = ((aa & 1) ? va1 : va0) + (aa >> 1) * 64; vf[0][aa] = trfrag(ad, ad + 8704); }
#pragma unroll
        for (int kk = 0; kk < 4; ++kk) {
            if (kk < 3) {
                wait_prog_c(nW + 16 + 4 * (kk + 2), seen);
#pragma unroll
                for (int aa = 0; aa < 4; ++aa) { const unsigned ad = ((aa & 1) ? va1 : va0) + (aa >> 1) * 64 + (kk + 1) * 17408; vf[(kk + 1) & 1][aa] = trfrag(ad, ad + 8704); }
            }
#pragma unroll
            for (int aa = 0; aa < 4; ++aa) { const f32x4 d = mfma(vf[kk & 1][aa], pf[kk], f32x4{0.f, 0.f, 0.f, 0.f}); o[aa] = kk == 0 ? fk[0] * d : o[aa] + fk[kk] * d; }
        }
        float v[4];
#pragma unroll
        for (int r = 0; r < 4; ++r) { const float lo = (n & 2) ? o[1][r] : o[0][r], hi = (n & 2) ? o[3][r] : o[2][r]; v[r] = (n & 4) ? hi : lo; }
        if (n < 8) stg(rs, ooff, cvtpk(v[0], v[1]), cvtpk(v[2], v[3]), T0, T1);
    }
    STAMP(5);
    if (owner) {
        const unsigned ba = L_K + (4 * g + r1) * 928 + t * 224 + c4 * 8;
        f32x4 acc[7];
        v4u wf[7];
        wait_prog(nW + 32 + 4);
        if (mlpo) {
            wait_lds_ge(F_AM, 4);
            const v2u am = lds64(L_AM + (n & 3) * 32 + g * 8);
            const v4u B = v4u{am.x, am.y, 0u, 0u};
#pragma unroll
            for (int T = 0; T < 7; ++T) { const v2u wl = tr64(ba + T * 32); acc[T] = mfma(v4u{wl.x, wl.y, 0u, 0u}, B, f32x4{0.f, 0.f, 0.f, 0.f}); }
        } else {
#pragma unroll
            for (int T = 0; T < 7; ++T) acc[T] = f32x4{0.f, 0.f, 0.f, 0.f};
        }
        const int h = j >> 3;
        const int oo = WS_O + ((own * 4 + t) * 64 + lane) * 16, so = WS_S + (((kvh * 2 + h) * 4 + t) * 8 + (lane >> 3)) * 64;
        const int foff0 = WS_FIN + ((((n & 3) * 28 + 7 * t + (n >> 2)) * 128 + own) * 4 + g) * 16;
        wait_prog(nW + 32 + 12);
#pragma unroll
        for (int T = 0; T < 7; ++T) wf[T] = trfrag(ba + 16 * 928 + T * 32, ba + 32 * 928 + T * 32);
        v4u og, sg;
#pragma unroll 1
        for (unsigned it = 0; it < SPIN_CAP; ++it) {
            cbar();
            og = ldg(rs, oo); sg = ldg(rs, so);
            if (__all(tags_ok(og, T0, T1) & tags_ok(sg, T0, T1))) break;
        }
        STAMP(6);
        const float m = __uint_as_float(sg.x), l = __uint_as_float(sg.z);
        const float M = xrow_max(fmaxf(m, dpp<ROR8>(m))), e = __builtin_amdgcn_exp2f(m - M);
        float le = l * e; le += dpp<ROR8>(le);
        const float wgt = e * __builtin_amdgcn_rcpf(xrow_sum(le));
        float v[4] = {bflo(og.x) * wgt, bfhi(og.x) * wgt, bflo(og.z) * wgt, bfhi(og.z) * wgt};
#pragma unroll
        for (int r = 0; r < 4; ++r) { v[r] += dpp<ROR8>(v[r]); v[r] = xrow_sum(v[r]); }
        if (lane < 8) *(LAS v2u*)(L_A + t * 64 + (8 * (lane & 3) + 4 * (lane >> 2)) * 2) = v2u{cvtpk(v[0], v[1]), cvtpk(v[2], v[3])};
        lds_signal(F_A);
        spin_lds_ge(F_A, 4);
        const v4u B = lds128(L_A + (n & 3) * 64 + g * 16);
#pragma unroll
        for (int T = 0; T < 7; ++T) acc[T] = mfma(wf[T], B, acc[T]);
        unsigned p0[7], p1[7];
#pragma unroll
        for (int T = 0; T < 7; ++T) { p0[T] = cvtpk(acc[T][0], acc[T][1]); p1[T] = cvtpk(acc[T][2], acc[T][3]); }
        const int q4 = n >> 2;
        const unsigned a0 = q4 == 0 ? p0[0] : q4 == 1 ? p0[1] : q4 == 2 ? p0[2] : p0[3], a1 = q4 == 0 ? p1[0] : q4 == 1 ? p1[1] : q4 == 2 ? p1[2] : p1[3];
        const unsigned c0 = q4 == 0 ? p0[4] : q4 == 1 ? p0[5] : p0[6], c1 = q4 == 0 ? p1[4] : q4 == 1 ? p1[5] : p1[6];
        stg(rs, foff0, a0, a1, T0, T1);
        if (q4 < 3) stg(rs, foff0 + 4 * 128 * 4 * 16, c0, c1, T0, T1);
    }
    STAMP(7);
    if (t == 3 && fin) {
        const int fo = WS_FIN + ((fb * 28 + fc) * 512 + lane) * 16;
        {
#pragma unroll 1
            for (unsigned it = 0; it < SPIN_CAP; ++it) {
                cbar();
                bool ok = false;
                if (lane < 2) ok = tags_ok(ldg(rs, WS_FIN + ((fb * 28 + fc) * 512 + (kvh * 16 + lane * 8) * 4) * 16), T0, T1);
                if (__any(ok)) break;
                __builtin_amdgcn_s_sleep(FPROBE);
            }
        }
        v4u f[8];
#pragma unroll 1
        for (unsigned it = 0; it < SPIN_CAP; ++it) {
            cbar();
            bool ok = true;
#pragma unroll
            for (int i = 0; i < 8; ++i) f[i] = ldg(rs, fo + i * 1024);
#pragma unroll
            for (int i = 0; i < 8; ++i) ok &= tags_ok(f[i], T0, T1);
            if (__all(ok)) break;
        }
        const v4u sel = v4u{n == 0 ? 0x00003F80u : n == 1 ? 0x3F800000u : 0u, 0u, n == 2 ? 0x00003F80u : n == 3 ? 0x3F800000u : 0u, 0u};
        f32x4 d0 = {0.f, 0.f, 0.f, 0.f}, d1 = {0.f, 0.f, 0.f, 0.f};
#pragma unroll
        for (int i = 0; i < 8; i += 2) { d0 = mfma(sel, f[i], d0); d1 = mfma(sel, f[i + 1], d1); }
        float v[4];
#pragma unroll
        for (int r = 0; r < 4; ++r) { float x = d0[r] + d1[r]; x += dpp<ROR8>(x); v[r] = x + dpp<ROR4>(x); }
        if (lane < 4) {
            asm volatile("" : "+v"(xres));
            *(GAS v2u*)(a.out + fb * 448 + 16 * fc + 4 * lane) = v2u{cvtpk(bflo(xres.x) + v[0], bfhi(xres.x) + v[1]), cvtpk(bflo(xres.y) + v[2], bfhi(xres.y) + v[3])};
        }
        if (blk == 128 && lane == 0) *(GAS unsigned*)(a.ws + WS_E) = Eu + 1u;
        STAMP(8);
    }
    STAMP_FLUSH();
}
}

extern "C" void kernel_launch(void* const* d_in, const int* in_sizes, int n_in, void* d_out, int out_size, void* d_ws, size_t ws_size, hipStream_t stream) {
    (void)in_sizes; (void)n_in; (void)out_size; (void)ws_size;
    fk::Args a;
    a.x = (const unsigned char*)d_in[0]; a.k = (const unsigned char*)d_in[1]; a.v = (const unsigned char*)d_in[2];
    a.wq = (const unsigned char*)d_in[3]; a.wo = (const unsigned char*)d_in[4]; a.w1 = (const unsigned char*)d_in[6];
    a.b1 = (const unsigned char*)d_in[7]; a.w2 = (const unsigned char*)d_in[8]; a.out = (unsigned short*)d_out; a.ws = (unsigned char*)d_ws;
    (void)hipFuncSetAttribute((const void*)fk::fused, hipFuncAttributeMaxDynamicSharedMemorySize, fk::LDS_BYTES);
    hipLaunchKernelGGL(fk::fused, dim3(256), dim3(512), fk::LDS_BYTES, stream, a);
}
```

```cpp
#include <hip/hip_runtime.h>
#include <hip/hip_bf16.h>
#include <stdint.h>

#define GAS __attribute__((address_space(1)))
#define LAS __attribute__((address_space(3)))
typedef unsigned v4u __attribute__((ext_vector_type(4)));
typedef unsigned v2u __attribute__((ext_vector_type(2)));
typedef float f32x4 __attribute__((ext_vector_type(4)));
typedef float f32x2 __attribute__((ext_vector_type(2)));
typedef short s16x4 __attribute__((ext_vector_type(4)));
typedef __bf16 bf16x8_t __attribute__((ext_vector_type(8)));
typedef __bf16 bf16x2_t __attribute__((ext_vector_type(2)));

#define STAMPS 0
#define SETPRIO 3
#define QPOLL 14
#define FPROBE 6
#define WINK 6
#define WINSW 4
#define BS 4
#define PROBE8 1
#define PROBEJ 15
#define PSLEEP 0
#define WINL 8
#define PREFETCH_CODE 2

namespace fk {
constexpr int AUX_SC1 = 16;
constexpr int L_K = 0;
constexpr int L_WQ = 67584;
constexpr int L_V = L_WQ + 16384;
constexpr int L_X = L_V + 69632 - 8192 - 4608;
constexpr int L_W1 = L_V + 69632 - 8192;
constexpr int L_M = L_V + 69632;
constexpr int L_Q = L_M;
constexpr int L_P = L_Q + 1024;
constexpr int L_A = L_P + 512;
constexpr int L_AM = L_A + 256;
constexpr int L_MX = L_AM + 128;
constexpr int L_LS = L_MX + 32;
constexpr int L_F = L_LS + 32;
constexpr int F_S1 = L_F + 16, F_QK = L_F + 20, F_M = L_F + 24, F_P = L_F + 28, F_Q = L_F + 32, F_A = L_F + 36, F_AM = L_F + 40;
constexpr int L_PF = L_F + 64;
constexpr int LDS_BYTES = L_PF + 1024;
static_assert(L_X >= L_V + 52 * 1088, "X / W1 images alias V instructions >= 52 only");
static_assert(48 * 928 <= 67584, "projection rows inside the K image");
static_assert(LDS_BYTES <= 163840, "LDS");
static_assert((L_F & 15) == 0 && (L_MX & 15) == 0 && (L_A & 15) == 0, "alignment");
constexpr int WS_E = 0;
constexpr int WS_Q = 4096;
constexpr int WS_H = WS_Q + 4 * 8 * 4 * 128 * 16;
constexpr int WS_O = WS_H + 4 * 7 * 448 * 16;
constexpr int WS_S = WS_O + 128 * 256 * 16;
constexpr int WS_FIN = WS_S + 512 * 64;
constexpr int WS_ST = WS_FIN + 4 * 28 * 512 * 16;
constexpr int WS_END = WS_ST + 256 * 8 * 16 * 8;
constexpr unsigned SPIN_CAP = 1u << 17;

struct Args {
    const unsigned char* x; const unsigned char* k; const unsigned char* v; const unsigned char* wq; const unsigned char* wo;
    const unsigned char* w1; const unsigned char* b1; const unsigned char* w2; unsigned short* out; unsigned char* ws;
};

__device__ __forceinline__ unsigned long long rt() { return __builtin_amdgcn_s_memrealtime(); }
template <bool NT> __device__ __forceinline__ void dma16(unsigned voff, unsigned long long sbase, unsigned lds) {
    if (NT) asm volatile("s_mov_b32 m0, %2\n\ts_nop 0\n\tglobal_load_lds_dwordx4 %0, %1 nt" :: "v"(voff), "s"(sbase), "s"(lds) : "memory");
    else    asm volatile("s_mov_b32 m0, %2\n\ts_nop 0\n\tglobal_load_lds_dwordx4 %0, %1" :: "v"(voff), "s"(sbase), "s"(lds) : "memory");
}
template <int N> __device__ __forceinline__ void waitv() { asm volatile("s_waitcnt vmcnt(%0)" :: "i"(N) : "memory"); }
__device__ __forceinline__ void cbar() { asm volatile("" ::: "memory"); }

__device__ __forceinline__ f32x4 mfma(v4u a, v4u b, f32x4 c) {
    return __builtin_amdgcn_mfma_f32_16x16x32_bf16(__builtin_bit_cast(bf16x8_t, a), __builtin_bit_cast(bf16x8_t, b), c, 0, 0, 0);
}
__device__ __forceinline__ v4u lds128(unsigned a) { return *(const LAS v4u*)a; }
__device__ __forceinline__ v2u lds64(unsigned a) { return *(const LAS v2u*)a; }
__device__ __forceinline__ v2u tr64(unsigned a) { return __builtin_bit_cast(v2u, __builtin_amdgcn_ds_read_tr16_b64_v4i16((LAS s16x4*)a)); }
__device__ __forceinline__ v4u trfrag(unsigned a0, unsigned a1) { v2u lo = tr64(a0), hi = tr64(a1); return v4u{lo.x, lo.y, hi.x, hi.y}; }
__device__ __forceinline__ unsigned cvtpk(float lo, float hi) { return __builtin_bit_cast(unsigned, __builtin_convertvector(f32x2{lo, hi}, bf16x2_t)); }
__device__ __forceinline__ float bflo(unsigned u) { return __builtin_bit_cast(float, u << 16); }
__device__ __forceinline__ float bfhi(unsigned u) { return __builtin_bit_cast(float, u & 0xffff0000u); }
template <int CTRL> __device__ __forceinline__ float dpp(float x) {
    return __builtin_bit_cast(float, __builtin_amdgcn_mov_dpp(__builtin_bit_cast(int, x), CTRL, 0xf, 0xf, true));
}
constexpr int XOR1 = 0xB1, XOR2 = 0x4E, HMIR = 0x141, ROR4 = 0x124, ROR8 = 0x128, NBC4 = 0x154;
__device__ __forceinline__ float xrow_max(float x) {
    auto s = __builtin_amdgcn_permlane16_swap(__float_as_uint(x), __float_as_uint(x), false, false);
    x = fmaxf(__uint_as_float(s[0]), __uint_as_float(s[1]));
    auto t = __builtin_amdgcn_permlane32_swap(__float_as_uint(x), __float_as_uint(x), false, false);
    return fmaxf(__uint_as_float(t[0]), __uint_as_float(t[1]));
}
__device__ __forceinline__ float x32_sum(float x) {
    auto t = __builtin_amdgcn_permlane32_swap(__float_as_uint(x), __float_as_uint(x), false, false);
    return __uint_as_float(t[0]) + __uint_as_float(t[1]);
}
__device__ __forceinline__ float x16_sum(float x) {
    auto s = __builtin_amdgcn_permlane16_swap(__float_as_uint(x), __float_as_uint(x), false, false);
    return __uint_as_float(s[0]) + __uint_as_float(s[1]);
}
__device__ __forceinline__ float xrow_sum(float x) {
    auto s = __builtin_amdgcn_permlane16_swap(__float_as_uint(x), __float_as_uint(x), false, false);
    return x32_sum(__uint_as_float(s[0]) + __uint_as_float(s[1]));
}
__device__ __forceinline__ float par16_sum(float x) { x += dpp<XOR2>(x); x += dpp<ROR4>(x); return x + dpp<ROR8>(x); }
__device__ __forceinline__ float bperm(int srclane, float v) { return __builtin_bit_cast(float, __builtin_amdgcn_ds_bpermute(srclane * 4, __builtin_bit_cast(int, v))); }

__device__ __forceinline__ void wait_lds_ge(unsigned addr, unsigned want) {
#pragma unroll 1
    for (unsigned i = 0; i < (1u << 22); ++i) { if ((unsigned)__builtin_amdgcn_readfirstlane(*(volatile LAS unsigned*)addr) >= want) break; __builtin_amdgcn_s_sleep(1); }
    cbar();
}
__device__ __forceinline__ void spin_lds_ge(unsigned addr, unsigned want) {
#pragma unroll 1
    for (unsigned i = 0; i < (1u << 24); ++i) { if ((unsigned)__builtin_amdgcn_readfirstlane(*(volatile LAS unsigned*)addr) >= want) break; }
    cbar();
}
__device__ __forceinline__ void wait_prog(unsigned want) {
#pragma unroll 1
    for (unsigned i = 0; i < (1u << 22); ++i) {
        const v4u p = *(volatile LAS v4u*)L_F;
        const unsigned m01 = p.x < p.y ? p.x : p.y, m23 = p.z < p.w ? p.z : p.w;
        if ((unsigned)__builtin_amdgcn_readfirstlane(m01 < m23 ? m01 : m23) >= want) break;
        __builtin_amdgcn_s_sleep(1);
    }
    cbar();
}
__device__ __forceinline__ void wait_prog_c(unsigned want, unsigned& seen) {
#pragma unroll 1
    for (unsigned i = 0; i < (1u << 24) && seen < want; ++i) {
        const v4u p = *(volatile LAS v4u*)L_F;
        const unsigned m01 = p.x < p.y ? p.x : p.y, m23 = p.z < p.w ? p.z : p.w;
        seen = (unsigned)__builtin_amdgcn_readfirstlane(m01 < m23 ? m01 : m23);
    }
    cbar();
}
__device__ __forceinline__ void lds_signal(unsigned addr) {
    asm volatile("s_waitcnt lgkmcnt(0)\n\ts_mov_b64 exec, 1\n\tds_add_u32 %0, %1\n\ts_mov_b64 exec, -1" :: "v"(addr), "v"(1u) : "memory");
}
__device__ __forceinline__ bool tags_ok(v4u g, unsigned t0, unsigned t1) { return (g.y == t0) & (g.w == t1); }
__device__ __forceinline__ v4u ldg(__amdgpu_buffer_rsrc_t rs, int off) { return __builtin_amdgcn_raw_buffer_load_b128(rs, off, 0, AUX_SC1); }
__device__ __forceinline__ void stg(__amdgpu_buffer_rsrc_t rs, int off, unsigned p0, unsigned p1, unsigned t0, unsigned t1) { __builtin_amdgcn_raw_buffer_store_b128(v4u{p0, t0, p1, t1}, rs, off, 0, AUX_SC1); }

#define STAMP(i) do { } while (0)
#define NPOLL(i, v) do { } while (0)
#define STAMP_DECL do { } while (0)
#define STAMP_FLUSH() do { } while (0)

__global__ void __launch_bounds__(512, 2) fused(Args a) {
    const int tid = threadIdx.x, lane = tid & 63, w = __builtin_amdgcn_readfirstlane(tid >> 6);
    const int blk = blockIdx.x, kvh = blk & 7, j = blk >> 3, b = j >> 3, s = j & 7;
    const bool hrole = blk < 196, owner = j < 16;
    const int own = kvh * 16 + j;
    const bool mlpo = owner && own < 112;
    const int nq = b < 2 ? 4 : 3;
    const unsigned nW = 1 + nq + (hrole ? 2 : 0);
    STAMP_DECL;
    STAMP(0);
    {
        unsigned long long pc;
        asm volatile("s_getpc_b64 %0" : "=s"(pc));
        pc = (pc & ~0xFFull) + w * 1536;
        dma16<false>(lane * 16, pc, L_PF);
        dma16<false>(lane * 16, pc + 1024, L_PF);
    }
    if (__builtin_expect(w < 4, 1)) {
        const int L = w;
        if (lane < 56) dma16<false>(lane * 16, (unsigned long long)a.x + L * 896, L_X + L * 912);
        const unsigned f = ((lane >> 4) & 1) | ((L & 1) << 1), sp = lane & 7, gp = (((sp >> 1) ^ f) << 1) | (sp & 1);
        {
            const int r0 = b < 2 ? b * 128 : 256 + (b - 2) * 96;
            unsigned long long p = (unsigned long long)a.wq + (size_t)(r0 + 8 * L) * 8192 + (kvh * 512 + s * 64) * 2; unsigned l = L_WQ + L * 1024;
            const unsigned voff = (lane >> 3) * 8192 + gp * 16;
#pragma unroll 1
            for (int i = 0; i < nq; ++i) { dma16<true>(voff, p, l); p += 32 * 8192; l += 4096; }
        }
        if (hrole) {
            const int cb = blk % 28, rb = blk / 28;
            unsigned long long p = (unsigned long long)a.w1 + (size_t)(rb * 64 + 8 * L) * 3584 + cb * 128;
            const unsigned voff = (lane >> 3) * 3584 + gp * 16;
            dma16<true>(voff, p, L_W1 + L * 1024);
            dma16<true>(voff, p + 32 * 3584, L_W1 + L * 1024 + 4096);
        }
        const size_t kvbase = ((size_t)(b * 8 + kvh) * 4096 + 3072) * 512 + (size_t)s * (BS * 1024) + L * 1024;
#define KVSTEP(i) ((((i) + 1) * 4 % BS == 0) ? (7 * BS + 4) * 1024 : 4096)
        unsigned n = nW;
        volatile LAS unsigned* prog = (volatile LAS unsigned*)(L_F + 4 * L);
        {
            const unsigned voff = (lane >> 5) * 512 + ((lane & 31) ^ (lane >> 5)) * 16;
            unsigned long long p = (unsigned long long)a.k + kvbase; unsigned l = L_K + L * 1056;
            dma16<true>(voff, p, l); ++n; p += KVSTEP(0); l += 4224;
            __builtin_amdgcn_s_barrier();
#pragma unroll 1
            for (int i = 1; i < 16; ++i) { dma16<true>(voff, p, l); ++n; waitv<WINK - 1>(); const int d = (int)n - (WINK - 1); *prog = d > 0 ? d : 0; p += KVSTEP(i); l += 4224; }
        }
        STAMP(1);
        {
            const unsigned voff = (lane >> 5) * 512 + ((lane & 31) ^ ((lane >> 5) << 1)) * 16;
            unsigned long long p = (unsigned long long)a.v + kvbase; unsigned l = L_V + L * 1088;
#pragma unroll 1
            for (int i = 0; i < 16; ++i) {
                if (i == 12) wait_lds_ge(F_S1, 4);
                dma16<true>(voff, p, l); ++n;
                if (i < WINSW) { waitv<WINK - 1>(); *prog = n - (WINK - 1); } else { waitv<WINL - 1>(); *prog = n - (WINL - 1); }
                p += KVSTEP(i); l += 4352;
            }
        }
        STAMP(2);
        if (owner) {
            wait_lds_ge(F_P, 4);
            const unsigned voff = lane * 16;
            unsigned l = L_K + L * 928;
            const unsigned long long w2p = (unsigned long long)a.w2 + (unsigned)(((mlpo ? own : 0) * 16 + L) * 896);
            const unsigned long long wop = (unsigned long long)a.wo + (unsigned)((own * 32 + L) * 896);
            if (lane < 56) {
#pragma unroll
                for (int i = 0; i < 4; ++i) dma16<true>(voff, w2p + i * 4 * 896, l + i * 3712);
            }
            waitv<8>(); *prog = n - 4;
            waitv<4>(); *prog = n;
            STAMP(4);
            if (lane < 56) {
#pragma unroll
                for (int i = 0; i < 8; ++i) dma16<true>(voff, wop + i * 4 * 896, l + (4 + i) * 3712);
            }
            waitv<8>(); *prog = n + 4;
            waitv<0>(); *prog = n + 12;
        } else {
            waitv<4>(); *prog = n - 4;
            waitv<0>(); *prog = n;
            STAMP(4);
        }
        STAMP(3);
        STAMP_FLUSH();
        return;
    }
    const int t = w - 4, n = lane & 15, g = lane >> 4;
    __builtin_amdgcn_s_setprio(SETPRIO);
    if (t == 0) {
        if (lane < 16) *(LAS unsigned*)(L_F + 4 * lane) = 0u;
        if (lane < 56) *(LAS v4u*)(L_X + 4 * 912 + lane * 16) = v4u{0x3F803F80u, 0x3F803F80u, 0x3F803F80u, 0x3F803F80u};
    }
    asm volatile("s_waitcnt lgkmcnt(0)" ::: "memory");
    __builtin_amdgcn_s_barrier();
    cbar();
    const __amdgpu_buffer_rsrc_t rs = __builtin_amdgcn_make_buffer_rsrc(a.ws, 0, WS_END, 0x00020000);
    const unsigned Eu = *(const __attribute__((address_space(4))) unsigned*)(a.ws + WS_E);
    const unsigned T0 = (Eu * 0x9E3779B1u + 0x7F4A7C15u) & 0xBFFFBFFFu, T1 = ((Eu ^ 0x5BD1E995u) * 0x85EBCA6Bu + 0x165667B1u) & 0xBFFFBFFFu;
    v2u b1v = v2u{0u, 0u};
    if (mlpo && lane < 32) b1v = *(const GAS v2u*)(a.b1 + (own * 16 + (lane & 3) * 4) * 2);
    const int fi = blk - 128, fb = fi / 28, fc = fi % 28;
    const bool fin = fi >= 0 && fi < 112;
    v2u xres = v2u{0u, 0u};
    if (t == 3 && fin && lane < 4) xres = *(const GAS v2u*)(a.x + (fb * 448 + 16 * fc + 4 * lane) * 2);
    const unsigned r1 = n >> 2, c4 = n & 3;

    {
        const unsigned xa = L_X + (n < 4 ? n : 4) * 912 + g * 16;
        const unsigned fs = ((r1 >> 1) & 1) | ((g & 1) << 1);
        const unsigned wo_ = (8 * g + r1) * 128 + ((t ^ fs) * 32) + c4 * 8;
        const int ks0 = b < 2 ? b * 4 : 8 + (b - 2) * 3, rb = blk / 28;
        const int qoff = WS_Q + (((n * 8 + kvh) * 4 + b) * 128 + s * 16 + t * 4 + g) * 16;
        const int hoff = WS_H + ((n * 7 + rb) * 448 + (blk % 28) * 16 + t * 4 + g) * 16;
        wait_prog(1);
        v4u xf[14];
#pragma unroll
        for (int ks = 0; ks < 14; ++ks) xf[ks] = lds128(xa + 64 * ks);
        v4u bq[4], bh[2];
#pragma unroll
        for (int kk = 0; kk < 4; ++kk) bq[kk] = lds128(xa + 64 * (ks0 + (kk < nq ? kk : 0)));
#pragma unroll
        for (int kk = 0; kk < 2; ++kk) bh[kk] = lds128(xa + 64 * ((hrole ? 2 * rb : 0) + kk));
        __builtin_amdgcn_sched_barrier(0);
        f32x4 G0 = {0.f, 0.f, 0.f, 0.f}, G1 = {0.f, 0.f, 0.f, 0.f};
#pragma unroll
        for (int ks = 0; ks < 14; ks += 2) { G0 = mfma(xf[ks], xf[ks], G0); G1 = mfma(xf[ks + 1], xf[ks + 1], G1); }
        const f32x4 G = G0 + G1;
        const float gsel = n == 0 ? G[0] : n == 1 ? G[1] : n == 2 ? G[2] : G[3];
        const float sq = bperm(n & 3, gsel), sm = bperm(16 + (n & 3), G[0]);
        const float mean = sm * (1.0f / 448.0f);
        const float var = fmaxf(fmaf(sq, 1.0f / 448.0f, -mean * mean), 0.f);
        const float rstd = __builtin_amdgcn_rsqf(var + 1e-5f), mr = mean * rstd;
        asm volatile("" :: "v"(rstd), "v"(mr), "v"(qoff), "v"(hoff));
        wait_prog(nW);
        STAMP(1);
        v4u aq[4], ah[2];
#pragma unroll
        for (int kk = 0; kk < 4; ++kk) aq[kk] = trfrag(L_WQ + wo_ + kk * 4096, L_WQ + wo_ + kk * 4096 + 512);
#pragma unroll
        for (int kk = 0; kk < 2; ++kk) ah[kk] = trfrag(L_W1 + wo_ + kk * 4096, L_W1 + wo_ + kk * 4096 + 512);
        __builtin_amdgcn_sched_barrier(0);
        f32x4 Dq = {0.f, 0.f, 0.f, 0.f}, Dq2 = {0.f, 0.f, 0.f, 0.f}, Dh = {0.f, 0.f, 0.f, 0.f};
        Dq = mfma(aq[0], bq[0], Dq);
        Dq2 = mfma(aq[1], bq[1], Dq2);
        Dh = mfma(ah[0], bh[0], Dh);
        Dq = mfma(aq[2], bq[2], Dq);
        if (nq == 4) Dq2 = mfma(aq[3], bq[3], Dq2);
        Dh = mfma(ah[1], bh[1], Dh);
        Dq += Dq2;
        lds_signal(F_S1);
        float q[4], h[4];
#pragma unroll
        for (int r = 0; r < 4; ++r) { q[r] = fmaf(rstd, Dq[r], -mr * dpp<NBC4>(Dq[r])); h[r] = fmaf(rstd, Dh[r], -mr * dpp<NBC4>(Dh[r])); }
        if (n < 4) {
            stg(rs, qoff, cvtpk(q[0], q[1]), cvtpk(q[2], q[3]), T0, T1);
            if (hrole) stg(rs, hoff, cvtpk(h[0], h[1]), cvtpk(h[2], h[3]), T0, T1);
        }
    }
    STAMP(2);
    const int gid = b * 8 + kvh;
    v4u hx = v4u{0u, 0u, 0u, 0u};
    const bool hval = mlpo && lane < 28;
    {
        const int qo = WS_Q + (gid * 512 + (lane >> 5) * 128 + 32 * t + (lane & 31)) * 16;
        const int ho = hval ? WS_H + ((t * 7 + (lane >> 2)) * 448 + own * 4 + (lane & 3)) * 16 : qo;
        v4u x0, x1;
        wait_prog(nW + QPOLL);
#pragma unroll 1
        for (unsigned it = 0; it < SPIN_CAP; ++it) {
            cbar();
            NPOLL(9, 1);
            x0 = ldg(rs, qo); x1 = ldg(rs, qo + 256 * 16); hx = ldg(rs, ho);
            if (__all(tags_ok(x0, T0, T1) & tags_ok(x1, T0, T1) & (!hval | tags_ok(hx, T0, T1)))) break;
        }
        STAMP(3);
        const float q0 = x32_sum(bflo(x0.x) + bflo(x1.x)), q1 = x32_sum(bfhi(x0.x) + bfhi(x1.x));
        const float q2 = x32_sum(bflo(x0.z) + bflo(x1.z)), q3 = x32_sum(bfhi(x0.z) + bfhi(x1.z));
        if (lane < 32) *(LAS v2u*)(L_Q + (32 * t + lane) * 8) = v2u{cvtpk(q0, q1), cvtpk(q2, q3)};
        lds_signal(F_Q);
    }
    {
        const unsigned ka = L_K + t * 16896 + (n >> 1) * 1056 + (n & 1) * 512 + ((g ^ (n & 1)) * 16);
        wait_prog(nW + 4 * (t + 1));
        v4u kf[16];
#pragma unroll
        for (int i = 0; i < 16; ++i) kf[i] = lds128(ka + (i >> 3) * 8448 + (i & 7) * 64);
        spin_lds_ge(F_Q, 4);
        v4u qf[8];
#pragma unroll
        for (int ks = 0; ks < 8; ++ks) qf[ks] = lds128(L_Q + (n & 1) * 512 + ks * 64 + g * 16);
        __builtin_amdgcn_sched_barrier(0);
        f32x4 z0 = {0.f, 0.f, 0.f, 0.f}, z1 = {0.f, 0.f, 0.f, 0.f};
#pragma unroll
        for (int ks = 0; ks < 8; ++ks) { z0 = mfma(kf[ks], qf[ks], z0); z1 = mfma(kf[8 + ks], qf[ks], z1); }
        constexpr float C = 0.0625f * 1.4426950408889634f;
        const float z[4] = {(n & 2) ? z1[0] : z0[0], (n & 2) ? z1[1] : z0[1], (n & 2) ? z1[2] : z0[2], (n & 2) ? z1[3] : z0[3]};
        float m = fmaxf(fmaxf(z[0], z[1]), fmaxf(z[2], z[3])) * C;
        m = xrow_max(fmaxf(m, dpp<XOR2>(m)));
        float p[4];
#pragma unroll
        for (int r = 0; r < 4; ++r) p[r] = __builtin_amdgcn_exp2f(fmaf(z[r], C, -m));
        if (n < 4) *(LAS v2u*)(L_P + (n & 1) * 256 + (32 * t + 8 * g + 4 * (n >> 1)) * 2) = v2u{cvtpk(p[0], p[1]), cvtpk(p[2], p[3])};
        float l = (p[0] + p[1]) + (p[2] + p[3]);
        l = xrow_sum(l + dpp<XOR2>(l));
        if (lane < 2) { *(LAS float*)(L_MX + lane * 16 + t * 4) = m; *(LAS float*)(L_LS + lane * 16 + t * 4) = l; }
        lds_signal(F_P);
    }
    STAMP(4);
    if (mlpo) {
        const float hv[4] = {hval ? bflo(hx.x) : 0.f, hval ? bfhi(hx.x) : 0.f, hval ? bflo(hx.z) : 0.f, hval ? bfhi(hx.z) : 0.f};
        const float bb[4] = {bflo(b1v.x), bfhi(b1v.x), bflo(b1v.y), bfhi(b1v.y)};
        float act[4];
#pragma unroll
        for (int r = 0; r < 4; ++r) {
            float v = hv[r]; v += dpp<ROR8>(v); v += dpp<ROR4>(v); v = x16_sum(v) + bb[r];
            const float e = __builtin_amdgcn_exp2f(-1.4426950408889634f * fabsf(v));
            const float num = v > 0.f ? fmaf(2.f, e, 1.f) : e * (e + 2.f);
            const float den = v > 0.f ? fmaf(2.f * e, e, num) : num + 2.f;
            act[r] = v * num * __builtin_amdgcn_rcpf(den);
        }
        if (lane < 4) *(LAS v2u*)(L_AM + t * 32 + lane * 8) = v2u{cvtpk(act[0], act[1]), cvtpk(act[2], act[3])};
        lds_signal(F_AM);
    }
    {
        const unsigned e = r1 & 1;
        const unsigned vb = L_V + (2 * g + (r1 >> 1)) * 1088 + e * 512 + t * 128 + c4 * 8;
        const unsigned va0 = vb + e * 32, va1 = vb + (e ^ 1) * 32;
        const int ooff = WS_O + ((((kvh * 16 + (n & 1) * 8 + 2 * t + ((n >> 2) & 1)) * 4 + b) * 8 + s) * 8 + 4 * ((n >> 1) & 1) + g) * 16;
        const int soff = WS_S + ((((kvh * 2 + (lane & 1)) * 4 + b) * 8 + s) * 4 + ((lane >> 1) & 3)) * 16;
        spin_lds_ge(F_P, 4);
        v4u pf[4];
#pragma unroll
        for (int kk = 0; kk < 4; ++kk) pf[kk] = lds128(L_P + (n & 1) * 256 + kk * 64 + g * 16);
        const v4u mx = lds128(L_MX + (n & 1) * 16), ls = lds128(L_LS + (n & 1) * 16);
        const float mk[4] = {__uint_as_float(mx.x), __uint_as_float(mx.y), __uint_as_float(mx.z), __uint_as_float(mx.w)};
        const float mW = fmaxf(fmaxf(mk[0], mk[1]), fmaxf(mk[2], mk[3]));
        float fk[4];
#pragma unroll
        for (int kk = 0; kk < 4; ++kk) fk[kk] = __builtin_amdgcn_exp2f(mk[kk] - mW);
        const float lW = fmaf(fk[0], __uint_as_float(ls.x), fk[1] * __uint_as_float(ls.y)) + fmaf(fk[2], __uint_as_float(ls.z), fk[3] * __uint_as_float(ls.w));
        if (t == 0 && lane < 8) stg(rs, soff, __float_as_uint(mW), __float_as_uint(lW), T0, T1);
        f32x4 o[4];
        unsigned seen = 0;
        v4u vf[2][4];
        wait_prog_c(nW + 20, seen);
#pragma unroll
        for (int aa = 0; aa < 4; ++aa) { const unsigned ad = ((aa & 1) ? va1 : va0) + (aa >> 1) * 64; vf[0][aa] = trfrag(ad, ad + 8704); }
#pragma unroll
        for (int kk = 0; kk < 4; ++kk) {
            if (kk < 3) {
                wait_prog_c(nW + 16 + 4 * (kk + 2), seen);
#pragma unroll
                for (int aa = 0; aa < 4; ++aa) { const unsigned ad = ((aa & 1) ? va1 : va0) + (aa >> 1) * 64 + (kk + 1) * 17408; vf[(kk + 1) & 1][aa] = trfrag(ad, ad + 8704); }
            }
#pragma unroll
            for (int aa = 0; aa < 4; ++aa) { const f32x4 d = mfma(vf[kk & 1][aa], pf[kk], f32x4{0.f, 0.f, 0.f, 0.f}); o[aa] = kk == 0 ? fk[0] * d : o[aa] + fk[kk] * d; }
        }
        float v[4];
#pragma unroll
        for (int r = 0; r < 4; ++r) { const float lo = (n & 2) ? o[1][r] : o[0][r], hi = (n & 2) ? o[3][r] : o[2][r]; v[r] = (n & 4) ? hi : lo; }
        if (n < 8) stg(rs, ooff, cvtpk(v[0], v[1]), cvtpk(v[2], v[3]), T0, T1);
    }
    STAMP(5);
    if (owner) {
        const unsigned ba = L_K + (4 * g + r1) * 928 + t * 224 + c4 * 8;
        f32x4 acc[7];
        v4u wf[7];
        wait_prog(nW + 32 + 4);
        if (mlpo) {
            wait_lds_ge(F_AM, 4);
            const v2u am = lds64(L_AM + (n & 3) * 32 + g * 8);
            const v4u B = v4u{am.x, am.y, 0u, 0u};
#pragma unroll
            for (int T = 0; T < 7; ++T) { const v2u wl = tr64(ba + T * 32); acc[T] = mfma(v4u{wl.x, wl.y, 0u, 0u}, B, f32x4{0.f, 0.f, 0.f, 0.f}); }
        } else {
#pragma unroll
            for (int T = 0; T < 7; ++T) acc[T] = f32x4{0.f, 0.f, 0.f, 0.f};
        }
        const int h = j >> 3;
        const int oo = WS_O + ((own * 4 + t) * 64 + lane) * 16, so = WS_S + (((kvh * 2 + h) * 4 + t) * 8 + (lane >> 3)) * 64;
        const int foff0 = WS_FIN + ((((n & 3) * 28 + 7 * t + (n >> 2)) * 128 + own) * 4 + g) * 16;
        wait_prog(nW + 32 + 12);
#pragma unroll
        for (int T = 0; T < 7; ++T) wf[T] = trfrag(ba + 16 * 928 + T * 32, ba + 32 * 928 + T * 32);
        v4u og, sg;
#pragma unroll 1
        for (unsigned it = 0; it < SPIN_CAP; ++it) {
            cbar();
            og = ldg(rs, oo); sg = ldg(rs, so);
            if (__all(tags_ok(og, T0, T1) & tags_ok(sg, T0, T1))) break;
        }
        STAMP(6);
        const float m = __uint_as_float(sg.x), l = __uint_as_float(sg.z);
        const float M = xrow_max(fmaxf(m, dpp<ROR8>(m))), e = __builtin_amdgcn_exp2f(m - M);
        float le = l * e; le += dpp<ROR8>(le);
        const float wgt = e * __builtin_amdgcn_rcpf(xrow_sum(le));
        float v[4] = {bflo(og.x) * wgt, bfhi(og.x) * wgt, bflo(og.z) * wgt, bfhi(og.z) * wgt};
#pragma unroll
        for (int r = 0; r < 4; ++r) { v[r] += dpp<ROR8>(v[r]); v[r] = xrow_sum(v[r]); }
        if (lane < 8) *(LAS v2u*)(L_A + t * 64 + (8 * (lane & 3) + 4 * (lane >> 2)) * 2) = v2u{cvtpk(v[0], v[1]), cvtpk(v[2], v[3])};
        lds_signal(F_A);
        spin_lds_ge(F_A, 4);
        const v4u B = lds128(L_A + (n & 3) * 64 + g * 16);
#pragma unroll
        for (int T = 0; T < 7; ++T) acc[T] = mfma(wf[T], B, acc[T]);
        unsigned p0[7], p1[7];
#pragma unroll
        for (int T = 0; T < 7; ++T) { p0[T] = cvtpk(acc[T][0], acc[T][1]); p1[T] = cvtpk(acc[T][2], acc[T][3]); }
        const int q4 = n >> 2;
        const unsigned a0 = q4 == 0 ? p0[0] : q4 == 1 ? p0[1] : q4 == 2 ? p0[2] : p0[3], a1 = q4 == 0 ? p1[0] : q4 == 1 ? p1[1] : q4 == 2 ? p1[2] : p1[3];
        const unsigned c0 = q4 == 0 ? p0[4] : q4 == 1 ? p0[5] : p0[6], c1 = q4 == 0 ? p1[4] : q4 == 1 ? p1[5] : p1[6];
        stg(rs, foff0, a0, a1, T0, T1);
        if (q4 < 3) stg(rs, foff0 + 4 * 128 * 4 * 16, c0, c1, T0, T1);
    }
    STAMP(7);
    if (t == 3 && fin) {
        const int fo = WS_FIN + ((fb * 28 + fc) * 512 + lane) * 16;
        {
#pragma unroll 1
            for (unsigned it = 0; it < SPIN_CAP; ++it) {
                cbar();
                bool ok = true;
                if (lane < 8) ok = tags_ok(ldg(rs, WS_FIN + ((fb * 28 + fc) * 512 + (lane * 16 + PROBEJ) * 4) * 16), T0, T1);
                if (__all(ok)) break;
                __builtin_amdgcn_s_sleep(FPROBE);
            }
            if (PSLEEP) __builtin_amdgcn_s_sleep(PSLEEP);
        }
        v4u f[8];
#pragma unroll 1
        for (unsigned it = 0; it < SPIN_CAP; ++it) {
            cbar();
            bool ok = true;
#pragma unroll
            for (int i = 0; i < 8; ++i) f[i] = ldg(rs, fo + i * 1024);
#pragma unroll
            for (int i = 0; i < 8; ++i) ok &= tags_ok(f[i], T0, T1);
            if (__all(ok)) break;
        }
        const v4u sel = v4u{n == 0 ? 0x00003F80u : n == 1 ? 0x3F800000u : 0u, 0u, n == 2 ? 0x00003F80u : n == 3 ? 0x3F800000u : 0u, 0u};
        f32x4 d0 = {0.f, 0.f, 0.f, 0.f}, d1 = {0.f, 0.f, 0.f, 0.f};
#pragma unroll
        for (int i = 0; i < 8; i += 2) { d0 = mfma(sel, f[i], d0); d1 = mfma(sel, f[i + 1], d1); }
        float v[4];
#pragma unroll
        for (int r = 0; r < 4; ++r) { float x = d0[r] + d1[r]; x += dpp<ROR8>(x); v[r] = x + dpp<ROR4>(x); }
        if (lane < 4) {
            asm volatile("" : "+v"(xres));
            *(GAS v2u*)(a.out + fb * 448 + 16 * fc + 4 * lane) = v2u{cvtpk(bflo(xres.x) + v[0], bfhi(xres.x) + v[1]), cvtpk(bflo(xres.y) + v[2], bfhi(xres.y) + v[3])};
        }
        if (blk == 128 && lane == 0) *(GAS unsigned*)(a.ws + WS_E) = Eu + 1u;
        STAMP(8);
    }
    STAMP_FLUSH();
}
}

extern "C" void kernel_launch(void* const* d_in, const int* in_sizes, int n_in, void* d_out, int out_size, void* d_ws, size_t ws_size, hipStream_t stream) {
    (void)in_sizes; (void)n_in; (void)out_size; (void)ws_size;
    fk::Args a;
    a.x = (const unsigned char*)d_in[0]; a.k = (const unsigned char*)d_in[1]; a.v = (const unsigned char*)d_in[2];
    a.wq = (const unsigned char*)d_in[3]; a.wo = (const unsigned char*)d_in[4]; a.w1 = (const unsigned char*)d_in[6];
    a.b1 = (const unsigned char*)d_in[7]; a.w2 = (const unsigned char*)d_in[8]; a.out = (unsigned short*)d_out; a.ws = (unsigned char*)d_ws;
    (void)hipFuncSetAttribute((const void*)fk::fused, hipFuncAttributeMaxDynamicSharedMemorySize, fk::LDS_BYTES);
    hipLaunchKernelGGL(fk::fused, dim3(256), dim3(512), fk::LDS_BYTES, stream, a);
}
```

```cpp
#include <hip/hip_runtime.h>
#include <hip/hip_bf16.h>
#include <stdint.h>

#define GAS __attribute__((address_space(1)))
#define LAS __attribute__((address_space(3)))
typedef unsigned v4u __attribute__((ext_vector_type(4)));
typedef unsigned v2u __attribute__((ext_vector_type(2)));
typedef float f32x4 __attribute__((ext_vector_type(4)));
typedef float f32x2 __attribute__((ext_vector_type(2)));
typedef short s16x4 __attribute__((ext_vector_type(4)));
typedef __bf16 bf16x8_t __attribute__((ext_vector_type(8)));
typedef __bf16 bf16x2_t __attribute__((ext_vector_type(2)));

#define STAMPS 0
#define SETPRIO 3
#define QPOLL 14
#define FPROBE 6
#define WINK 6
#define WINSW 4
#define BS 4
#define PROBE8 1
#define PROBEJ 15
#define PSLEEP 0
#define OSLEEP 0
#define WINL 8
#define PREFETCH_CODE 2

namespace fk {
constexpr int AUX_SC1 = 16;
constexpr int L_K = 0;
constexpr int L_WQ = 67584;
constexpr int L_V = L_WQ + 16384;
constexpr int L_X = L_V + 69632 - 8192 - 4608;
constexpr int L_W1 = L_V + 69632 - 8192;
constexpr int L_M = L_V + 69632;
constexpr int L_Q = L_M;
constexpr int L_P = L_Q + 1024;
constexpr int L_A = L_P + 512;
constexpr int L_AM = L_A + 256;
constexpr int L_MX = L_AM + 128;
constexpr int L_LS = L_MX + 32;
constexpr int L_Z = L_LS + 32;
constexpr int L_F = L_Z + 16;
constexpr int F_S1 = L_F + 16, F_QK = L_F + 20, F_M = L_F + 24, F_P = L_F + 28, F_Q = L_F + 32, F_A = L_F + 36, F_AM = L_F + 40;
constexpr int L_PF = L_F + 64;
constexpr int LDS_BYTES = L_PF + 1024;
static_assert(L_X >= L_V + 52 * 1088, "X / W1 images alias V instructions >= 52 only");
static_assert(48 * 928 <= 67584, "projection rows inside the K image");
static_assert(LDS_BYTES <= 163840, "LDS");
static_assert((L_F & 15) == 0 && (L_MX & 15) == 0 && (L_A & 15) == 0 && (L_Z & 15) == 0, "alignment");
constexpr int WS_E = 0;
constexpr int WS_Q = 4096;
constexpr int WS_H = WS_Q + 4 * 8 * 4 * 128 * 16;
constexpr int WS_O = WS_H + 4 * 7 * 448 * 16;
constexpr int WS_S = WS_O + 128 * 256 * 16;
constexpr int WS_FIN = WS_S + 512 * 64;
constexpr int WS_ST = WS_FIN + 4 * 28 * 512 * 16;
constexpr int WS_END = WS_ST + 256 * 8 * 16 * 8;
constexpr unsigned SPIN_CAP = 1u << 17;

struct Args {
    const unsigned char* x; const unsigned char* k; const unsigned char* v; const unsigned char* wq; const unsigned char* wo;
    const unsigned char* w1; const unsigned char* b1; const unsigned char* w2; unsigned short* out; unsigned char* ws;
};

__device__ __forceinline__ unsigned long long rt() { return __builtin_amdgcn_s_memrealtime(); }
template <bool NT> __device__ __forceinline__ void dma16(unsigned voff, unsigned long long sbase, unsigned lds) {
    if (NT) asm volatile("s_mov_b32 m0, %2\n\ts_nop 0\n\tglobal_load_lds_dwordx4 %0, %1 nt" :: "v"(voff), "s"(sbase), "s"(lds) : "memory");
    else    asm volatile("s_mov_b32 m0, %2\n\ts_nop 0\n\tglobal_load_lds_dwordx4 %0, %1" :: "v"(voff), "s"(sbase), "s"(lds) : "memory");
}
template <int N> __device__ __forceinline__ void waitv() { asm volatile("s_waitcnt vmcnt(%0)" :: "i"(N) : "memory"); }
__device__ __forceinline__ void cbar() { asm volatile("" ::: "memory"); }

__device__ __forceinline__ f32x4 mfma(v4u a, v4u b, f32x4 c) {
    return __builtin_amdgcn_mfma_f32_16x16x32_bf16(__builtin_bit_cast(bf16x8_t, a), __builtin_bit_cast(bf16x8_t, b), c, 0, 0, 0);
}
__device__ __forceinline__ v4u lds128(unsigned a) { return *(const LAS v4u*)a; }
__device__ __forceinline__ v2u lds64(unsigned a) { return *(const LAS v2u*)a; }
__device__ __forceinline__ v2u tr64(unsigned a) { return __builtin_bit_cast(v2u, __builtin_amdgcn_ds_read_tr16_b64_v4i16((LAS s16x4*)a)); }
__device__ __forceinline__ v4u trfrag(unsigned a0, unsigned a1) { v2u lo = tr64(a0), hi = tr64(a1); return v4u{lo.x, lo.y, hi.x, hi.y}; }
__device__ __forceinline__ unsigned cvtpk(float lo, float hi) { return __builtin_bit_cast(unsigned, __builtin_convertvector(f32x2{lo, hi}, bf16x2_t)); }
__device__ __forceinline__ float bflo(unsigned u) { return __builtin_bit_cast(float, u << 16); }
__device__ __forceinline__ float bfhi(unsigned u) { return __builtin_bit_cast(float, u & 0xffff0000u); }
template <int CTRL> __device__ __forceinline__ float dpp(float x) {
    return __builtin_bit_cast(float, __builtin_amdgcn_mov_dpp(__builtin_bit_cast(int, x), CTRL, 0xf, 0xf, true));
}
constexpr int XOR1 = 0xB1, XOR2 = 0x4E, HMIR = 0x141, ROR4 = 0x124, ROR8 = 0x128, NBC4 = 0x154;
__device__ __forceinline__ float xrow_max(float x) {
    auto s = __builtin_amdgcn_permlane16_swap(__float_as_uint(x), __float_as_uint(x), false, false);
    x = fmaxf(__uint_as_float(s[0]), __uint_as_float(s[1]));
    auto t = __builtin_amdgcn_permlane32_swap(__float_as_uint(x), __float_as_uint(x), false, false);
    return fmaxf(__uint_as_float(t[0]), __uint_as_float(t[1]));
}
__device__ __forceinline__ float x32_sum(float x) {
    auto t = __builtin_amdgcn_permlane32_swap(__float_as_uint(x), __float_as_uint(x), false, false);
    return __uint_as_float(t[0]) + __uint_as_float(t[1]);
}
__device__ __forceinline__ float x16_sum(float x) {
    auto s = __builtin_amdgcn_permlane16_swap(__float_as_uint(x), __float_as_uint(x), false, false);
    return __uint_as_float(s[0]) + __uint_as_float(s[1]);
}
__device__ __forceinline__ float xrow_sum(float x) {
    auto s = __builtin_amdgcn_permlane16_swap(__float_as_uint(x), __float_as_uint(x), false, false);
    return x32_sum(__uint_as_float(s[0]) + __uint_as_float(s[1]));
}
__device__ __forceinline__ float par16_sum(float x) { x += dpp<XOR2>(x); x += dpp<ROR4>(x); return x + dpp<ROR8>(x); }
__device__ __forceinline__ float bperm(int srclane, float v) { return __builtin_bit_cast(float, __builtin_amdgcn_ds_bpermute(srclane * 4, __builtin_bit_cast(int, v))); }

__device__ __forceinline__ void wait_lds_ge(unsigned addr, unsigned want) {
#pragma unroll 1
    for (unsigned i = 0; i < (1u << 22); ++i) { if ((unsigned)__builtin_amdgcn_readfirstlane(*(volatile LAS unsigned*)addr) >= want) break; __builtin_amdgcn_s_sleep(1); }
    cbar();
}
__device__ __forceinline__ void spin_lds_ge(unsigned addr, unsigned want) {
#pragma unroll 1
    for (unsigned i = 0; i < (1u << 24); ++i) { if ((unsigned)__builtin_amdgcn_readfirstlane(*(volatile LAS unsigned*)addr) >= want) break; }
    cbar();
}
__device__ __forceinline__ void wait_prog(unsigned want) {
#pragma unroll 1
    for (unsigned i = 0; i < (1u << 22); ++i) {
        const v4u p = *(volatile LAS v4u*)L_F;
        const unsigned m01 = p.x < p.y ? p.x : p.y, m23 = p.z < p.w ? p.z : p.w;
        if ((unsigned)__builtin_amdgcn_readfirstlane(m01 < m23 ? m01 : m23) >= want) break;
        __builtin_amdgcn_s_sleep(1);
    }
    cbar();
}
__device__ __forceinline__ void wait_prog_c(unsigned want, unsigned& seen) {
#pragma unroll 1
    for (unsigned i = 0; i < (1u << 24) && seen < want; ++i) {
        const v4u p = *(volatile LAS v4u*)L_F;
        const unsigned m01 = p.x < p.y ? p.x : p.y, m23 = p.z < p.w ? p.z : p.w;
        seen = (unsigned)__builtin_amdgcn_readfirstlane(m01 < m23 ? m01 : m23);
    }
    cbar();
}
__device__ __forceinline__ void lds_signal(unsigned addr) {
    asm volatile("s_waitcnt lgkmcnt(0)\n\ts_mov_b64 exec, 1\n\tds_add_u32 %0, %1\n\ts_mov_b64 exec, -1" :: "v"(addr), "v"(1u) : "memory");
}
__device__ __forceinline__ bool tags_ok(v4u g, unsigned t0, unsigned t1) { return (g.y == t0) & (g.w == t1); }
__device__ __forceinline__ v4u ldg(__amdgpu_buffer_rsrc_t rs, int off) { return __builtin_amdgcn_raw_buffer_load_b128(rs, off, 0, AUX_SC1); }
__device__ __forceinline__ void stg(__amdgpu_buffer_rsrc_t rs, int off, unsigned p0, unsigned p1, unsigned t0, unsigned t1) { __builtin_amdgcn_raw_buffer_store_b128(v4u{p0, t0, p1, t1}, rs, off, 0, AUX_SC1); }

#define STAMP(i) do { } while (0)
#define NPOLL(i, v) do { } while (0)
#define STAMP_DECL do { } while (0)
#define STAMP_FLUSH() do { } while (0)

__global__ void __launch_bounds__(512, 2) fused(Args a) {
    const int tid = threadIdx.x, lane = tid & 63, w = __builtin_amdgcn_readfirstlane(tid >> 6);
    const int blk = blockIdx.x, kvh = blk & 7, j = blk >> 3, b = j >> 3, s = j & 7;
    const bool hrole = blk < 196, owner = j < 16;
    const int own = kvh * 16 + j;
    const bool mlpo = owner && own < 112;
    const int nq = b < 2 ? 4 : 3;
    const unsigned nW = 1 + nq + (hrole ? 2 : 0);
    STAMP_DECL;
    STAMP(0);
    {
        unsigned long long pc;
        asm volatile("s_getpc_b64 %0" : "=s"(pc));
        pc = (pc & ~0xFFull) + w * 1536;
        dma16<false>(lane * 16, pc, L_PF);
        dma16<false>(lane * 16, pc + 1024, L_PF);
    }
    if (__builtin_expect(w < 4, 1)) {
        const int L = w;
        if (lane < 56) dma16<false>(lane * 16, (unsigned long long)a.x + L * 896, L_X + L * 912);
        const unsigned f = ((lane >> 4) & 1) | ((L & 1) << 1), sp = lane & 7, gp = (((sp >> 1) ^ f) << 1) | (sp & 1);
        {
            const int r0 = b < 2 ? b * 128 : 256 + (b - 2) * 96;
            unsigned long long p = (unsigned long long)a.wq + (size_t)(r0 + 8 * L) * 8192 + (kvh * 512 + s * 64) * 2; unsigned l = L_WQ + L * 1024;
            const unsigned voff = (lane >> 3) * 8192 + gp * 16;
#pragma unroll 1
            for (int i = 0; i < nq; ++i) { dma16<true>(voff, p, l); p += 32 * 8192; l += 4096; }
        }
        if (hrole) {
            const int cb = blk % 28, rb = blk / 28;
            unsigned long long p = (unsigned long long)a.w1 + (size_t)(rb * 64 + 8 * L) * 3584 + cb * 128;
            const unsigned voff = (lane >> 3) * 3584 + gp * 16;
            dma16<true>(voff, p, L_W1 + L * 1024);
            dma16<true>(voff, p + 32 * 3584, L_W1 + L * 1024 + 4096);
        }
        const size_t kvbase = ((size_t)(b * 8 + kvh) * 4096 + 3072) * 512 + (size_t)s * (BS * 1024) + L * 1024;
#define KVSTEP(i) ((((i) + 1) * 4 % BS == 0) ? (7 * BS + 4) * 1024 : 4096)
        unsigned n = nW;
        volatile LAS unsigned* prog = (volatile LAS unsigned*)(L_F + 4 * L);
        {
            const unsigned voff = (lane >> 5) * 512 + ((lane & 31) ^ (lane >> 5)) * 16;
            unsigned long long p = (unsigned long long)a.k + kvbase; unsigned l = L_K + L * 1056;
            dma16<true>(voff, p, l); ++n; p += KVSTEP(0); l += 4224;
            __builtin_amdgcn_s_barrier();
#pragma unroll 1
            for (int i = 1; i < 16; ++i) { dma16<true>(voff, p, l); ++n; waitv<WINK - 1>(); const int d = (int)n - (WINK - 1); *prog = d > 0 ? d : 0; p += KVSTEP(i); l += 4224; }
        }
        STAMP(1);
        {
            const unsigned voff = (lane >> 5) * 512 + ((lane & 31) ^ ((lane >> 5) << 1)) * 16;
            unsigned long long p = (unsigned long long)a.v + kvbase; unsigned l = L_V + L * 1088;
#pragma unroll 1
            for (int i = 0; i < 16; ++i) {
                if (i == 12) wait_lds_ge(F_S1, 4);
                dma16<true>(voff, p, l); ++n;
                if (i < WINSW) { waitv<WINK - 1>(); *prog = n - (WINK - 1); } else { waitv<WINL - 1>(); *prog = n - (WINL - 1); }
                p += KVSTEP(i); l += 4352;
            }
        }
        STAMP(2);
        if (owner) {
            wait_lds_ge(F_P, 4);
            const unsigned voff = lane * 16;
            unsigned l = L_K + L * 928;
            const unsigned long long w2p = (unsigned long long)a.w2 + (unsigned)(((mlpo ? own : 0) * 16 + L) * 896);
            const unsigned long long wop = (unsigned long long)a.wo + (unsigned)((own * 32 + L) * 896);
            if (lane < 56) {
#pragma unroll
                for (int i = 0; i < 4; ++i) dma16<true>(voff, w2p + i * 4 * 896, l + i * 3712);
            }
            waitv<8>(); *prog = n - 4;
            waitv<4>(); *prog = n;
            STAMP(4);
            if (lane < 56) {
#pragma unroll
                for (int i = 0; i < 8; ++i) dma16<true>(voff, wop + i * 4 * 896, l + (4 + i) * 3712);
            }
            waitv<8>(); *prog = n + 4;
            waitv<0>(); *prog = n + 12;
        } else {
            waitv<4>(); *prog = n - 4;
            waitv<0>(); *prog = n;
            STAMP(4);
        }
        STAMP(3);
        STAMP_FLUSH();
        return;
    }
    const int t = w - 4, n = lane & 15, g = lane >> 4;
    __builtin_amdgcn_s_setprio(SETPRIO);
    if (t == 0) {
        if (lane < 20) *(LAS unsigned*)(L_Z + 4 * lane) = 0u;
        if (lane < 56) *(LAS v4u*)(L_X + 4 * 912 + lane * 16) = v4u{0x3F803F80u, 0x3F803F80u, 0x3F803F80u, 0x3F803F80u};
    }
    asm volatile("s_waitcnt lgkmcnt(0)" ::: "memory");
    __builtin_amdgcn_s_barrier();
    cbar();
    const __amdgpu_buffer_rsrc_t rs = __builtin_amdgcn_make_buffer_rsrc(a.ws, 0, WS_END, 0x00020000);
    const unsigned Eu = *(const __attribute__((address_space(4))) unsigned*)(a.ws + WS_E);
    const unsigned T0 = (Eu * 0x9E3779B1u + 0x7F4A7C15u) & 0xBFFFBFFFu, T1 = ((Eu ^ 0x5BD1E995u) * 0x85EBCA6Bu + 0x165667B1u) & 0xBFFFBFFFu;
    v2u b1v = v2u{0u, 0u};
    if (mlpo && lane < 32) b1v = *(const GAS v2u*)(a.b1 + (own * 16 + (lane & 3) * 4) * 2);
    const int fi = blk - 128, fb = fi / 28, fc = fi % 28;
    const bool fin = fi >= 0 && fi < 112;
    v2u xres = v2u{0u, 0u};
    if (t == 3 && fin && lane < 4) xres = *(const GAS v2u*)(a.x + (fb * 448 + 16 * fc + 4 * lane) * 2);
    const unsigned r1 = n >> 2, c4 = n & 3;

    {
        const unsigned xa = L_X + (n < 4 ? n : 4) * 912 + g * 16;
        const unsigned fs = ((r1 >> 1) & 1) | ((g & 1) << 1);
        const unsigned wo_ = (8 * g + r1) * 128 + ((t ^ fs) * 32) + c4 * 8;
        const int ks0 = b < 2 ? b * 4 : 8 + (b - 2) * 3, rb = blk / 28;
        const int qoff = WS_Q + (((n * 8 + kvh) * 4 + b) * 128 + s * 16 + t * 4 + g) * 16;
        const int hoff = WS_H + ((n * 7 + rb) * 448 + (blk % 28) * 16 + t * 4 + g) * 16;
        wait_prog(1);
        v4u xf[14];
#pragma unroll
        for (int ks = 0; ks < 14; ++ks) xf[ks] = lds128(xa + 64 * ks);
        v4u bq[4], bh[2];
#pragma unroll
        for (int kk = 0; kk < 4; ++kk) bq[kk] = lds128(xa + 64 * (ks0 + (kk < nq ? kk : 0)));
#pragma unroll
        for (int kk = 0; kk < 2; ++kk) bh[kk] = lds128(xa + 64 * ((hrole ? 2 * rb : 0) + kk));
        __builtin_amdgcn_sched_barrier(0);
        f32x4 G0 = {0.f, 0.f, 0.f, 0.f}, G1 = {0.f, 0.f, 0.f, 0.f};
#pragma unroll
        for (int ks = 0; ks < 14; ks += 2) { G0 = mfma(xf[ks], xf[ks], G0); G1 = mfma(xf[ks + 1], xf[ks + 1], G1); }
        const f32x4 G = G0 + G1;
        const float gsel = n == 0 ? G[0] : n == 1 ? G[1] : n == 2 ? G[2] : G[3];
        const float sq = bperm(n & 3, gsel), sm = bperm(16 + (n & 3), G[0]);
        const float mean = sm * (1.0f / 448.0f);
        const float var = fmaxf(fmaf(sq, 1.0f / 448.0f, -mean * mean), 0.f);
        const float rstd = __builtin_amdgcn_rsqf(var + 1e-5f), mr = mean * rstd;
        asm volatile("" :: "v"(rstd), "v"(mr), "v"(qoff), "v"(hoff));
        wait_prog(nW);
        STAMP(1);
        v4u aq[4], ah[2];
#pragma unroll
        for (int kk = 0; kk < 4; ++kk) aq[kk] = trfrag(L_WQ + wo_ + kk * 4096, L_WQ + wo_ + kk * 4096 + 512);
#pragma unroll
        for (int kk = 0; kk < 2; ++kk) ah[kk] = trfrag(L_W1 + wo_ + kk * 4096, L_W1 + wo_ + kk * 4096 + 512);
        __builtin_amdgcn_sched_barrier(0);
        f32x4 Dq = {0.f, 0.f, 0.f, 0.f}, Dq2 = {0.f, 0.f, 0.f, 0.f}, Dh = {0.f, 0.f, 0.f, 0.f};
        Dq = mfma(aq[0], bq[0], Dq);
        Dq2 = mfma(aq[1], bq[1], Dq2);
        Dh = mfma(ah[0], bh[0], Dh);
        Dq = mfma(aq[2], bq[2], Dq);
        if (nq == 4) Dq2 = mfma(aq[3], bq[3], Dq2);
        Dh = mfma(ah[1], bh[1], Dh);
        Dq += Dq2;
        lds_signal(F_S1);
        float q[4], h[4];
#pragma unroll
        for (int r = 0; r < 4; ++r) { q[r] = fmaf(rstd, Dq[r], -mr * dpp<NBC4>(Dq[r])); h[r] = fmaf(rstd, Dh[r], -mr * dpp<NBC4>(Dh[r])); }
        if (n < 4) {
            stg(rs, qoff, cvtpk(q[0], q[1]), cvtpk(q[2], q[3]), T0, T1);
            if (hrole) stg(rs, hoff, cvtpk(h[0], h[1]), cvtpk(h[2], h[3]), T0, T1);
        }
    }
    STAMP(2);
    const int gid = b * 8 + kvh;
    v4u hx = v4u{0u, 0u, 0u, 0u};
    const bool hval = mlpo && lane < 28;
    {
        const int qo = WS_Q + (gid * 512 + (lane >> 5) * 128 + 32 * t + (lane & 31)) * 16;
        const int ho = hval ? WS_H + ((t * 7 + (lane >> 2)) * 448 + own * 4 + (lane & 3)) * 16 : qo;
        v4u x0, x1;
        wait_prog(nW + QPOLL);
#pragma unroll 1
        for (unsigned it = 0; it < SPIN_CAP; ++it) {
            cbar();
            NPOLL(9, 1);
            x0 = ldg(rs, qo); x1 = ldg(rs, qo + 256 * 16); hx = ldg(rs, ho);
            if (__all(tags_ok(x0, T0, T1) & tags_ok(x1, T0, T1) & (!hval | tags_ok(hx, T0, T1)))) break;
        }
        STAMP(3);
        const float q0 = x32_sum(bflo(x0.x) + bflo(x1.x)), q1 = x32_sum(bfhi(x0.x) + bfhi(x1.x));
        const float q2 = x32_sum(bflo(x0.z) + bflo(x1.z)), q3 = x32_sum(bfhi(x0.z) + bfhi(x1.z));
        if (lane < 32) *(LAS v2u*)(L_Q + (32 * t + lane) * 8) = v2u{cvtpk(q0, q1), cvtpk(q2, q3)};
        lds_signal(F_Q);
    }
    {
        const unsigned ka = L_K + t * 16896 + (n >> 1) * 1056 + (n & 1) * 512 + ((g ^ (n & 1)) * 16);
        wait_prog(nW + 4 * (t + 1));
        v4u kf[16];
#pragma unroll
        for (int i = 0; i < 16; ++i) kf[i] = lds128(ka + (i >> 3) * 8448 + (i & 7) * 64);
        spin_lds_ge(F_Q, 4);
        v4u qf[8];
#pragma unroll
        for (int ks = 0; ks < 8; ++ks) qf[ks] = lds128(L_Q + (n & 1) * 512 + ks * 64 + g * 16);
        __builtin_amdgcn_sched_barrier(0);
        f32x4 z0 = {0.f, 0.f, 0.f, 0.f}, z1 = {0.f, 0.f, 0.f, 0.f};
#pragma unroll
        for (int ks = 0; ks < 8; ++ks) { z0 = mfma(kf[ks], qf[ks], z0); z1 = mfma(kf[8 + ks], qf[ks], z1); }
        constexpr float C = 0.0625f * 1.4426950408889634f;
        const float z[4] = {(n & 2) ? z1[0] : z0[0], (n & 2) ? z1[1] : z0[1], (n & 2) ? z1[2] : z0[2], (n & 2) ? z1[3] : z0[3]};
        float m = fmaxf(fmaxf(z[0], z[1]), fmaxf(z[2], z[3])) * C;
        m = xrow_max(fmaxf(m, dpp<XOR2>(m)));
        float p[4];
#pragma unroll
        for (int r = 0; r < 4; ++r) p[r] = __builtin_amdgcn_exp2f(fmaf(z[r], C, -m));
        if (n < 4) *(LAS v2u*)(L_P + (n & 1) * 256 + (32 * t + 8 * g + 4 * (n >> 1)) * 2) = v2u{cvtpk(p[0], p[1]), cvtpk(p[2], p[3])};
        float l = (p[0] + p[1]) + (p[2] + p[3]);
        l = xrow_sum(l + dpp<XOR2>(l));
        if (lane < 2) { *(LAS float*)(L_MX + lane * 16 + t * 4) = m; *(LAS float*)(L_LS + lane * 16 + t * 4) = l; }
        lds_signal(F_P);
    }
    STAMP(4);
    if (mlpo) {
        const float hv[4] = {hval ? bflo(hx.x) : 0.f, hval ? bfhi(hx.x) : 0.f, hval ? bflo(hx.z) : 0.f, hval ? bfhi(hx.z) : 0.f};
        const float bb[4] = {bflo(b1v.x), bfhi(b1v.x), bflo(b1v.y), bfhi(b1v.y)};
        float act[4];
#pragma unroll
        for (int r = 0; r < 4; ++r) {
            float v = hv[r]; v += dpp<ROR8>(v); v += dpp<ROR4>(v); v = x16_sum(v) + bb[r];
            const float e = __builtin_amdgcn_exp2f(-1.4426950408889634f * fabsf(v));
            const float num = v > 0.f ? fmaf(2.f, e, 1.f) : e * (e + 2.f);
            const float den = v > 0.f ? fmaf(2.f * e, e, num) : num + 2.f;
            act[r] = v * num * __builtin_amdgcn_rcpf(den);
        }
        if (lane < 4) *(LAS v2u*)(L_AM + t * 32 + lane * 8) = v2u{cvtpk(act[0], act[1]), cvtpk(act[2], act[3])};
        lds_signal(F_AM);
    }
    {
        const unsigned e = r1 & 1;
        const unsigned vb = L_V + (2 * g + (r1 >> 1)) * 1088 + e * 512 + t * 128 + c4 * 8;
        const unsigned va0 = vb + e * 32, va1 = vb + (e ^ 1) * 32;
        const int ooff = WS_O + ((((kvh * 16 + (n & 1) * 8 + 2 * t + ((n >> 2) & 1)) * 4 + b) * 8 + s) * 8 + 4 * ((n >> 1) & 1) + g) * 16;
        const int soff = WS_S + ((((kvh * 2 + (lane & 1)) * 4 + b) * 8 + s) * 4 + ((lane >> 1) & 3)) * 16;
        spin_lds_ge(F_P, 4);
        v4u pf[4];
#pragma unroll
        for (int kk = 0; kk < 4; ++kk) pf[kk] = lds128(L_P + (n & 1) * 256 + kk * 64 + g * 16);
        const v4u mx = lds128(L_MX + (n & 1) * 16), ls = lds128(L_LS + (n & 1) * 16);
        const float mk[4] = {__uint_as_float(mx.x), __uint_as_float(mx.y), __uint_as_float(mx.z), __uint_as_float(mx.w)};
        const float mW = fmaxf(fmaxf(mk[0], mk[1]), fmaxf(mk[2], mk[3]));
        float fk[4];
#pragma unroll
        for (int kk = 0; kk < 4; ++kk) fk[kk] = __builtin_amdgcn_exp2f(mk[kk] - mW);
        const float lW = fmaf(fk[0], __uint_as_float(ls.x), fk[1] * __uint_as_float(ls.y)) + fmaf(fk[2], __uint_as_float(ls.z), fk[3] * __uint_as_float(ls.w));
        if (t == 0 && lane < 8) stg(rs, soff, __float_as_uint(mW), __float_as_uint(lW), T0, T1);
        f32x4 o[4];
        unsigned seen = 0;
        v4u vf[2][4];
        wait_prog_c(nW + 20, seen);
#pragma unroll
        for (int aa = 0; aa < 4; ++aa) { const unsigned ad = ((aa & 1) ? va1 : va0) + (aa >> 1) * 64; vf[0][aa] = trfrag(ad, ad + 8704); }
#pragma unroll
        for (int kk = 0; kk < 4; ++kk) {
            if (kk < 3) {
                wait_prog_c(nW + 16 + 4 * (kk + 2), seen);
#pragma unroll
                for (int aa = 0; aa < 4; ++aa) { const unsigned ad = ((aa & 1) ? va1 : va0) + (aa >> 1) * 64 + (kk + 1) * 17408; vf[(kk + 1) & 1][aa] = trfrag(ad, ad + 8704); }
            }
#pragma unroll
            for (int aa = 0; aa < 4; ++aa) { const f32x4 d = mfma(vf[kk & 1][aa], pf[kk], f32x4{0.f, 0.f, 0.f, 0.f}); o[aa] = kk == 0 ? fk[0] * d : o[aa] + fk[kk] * d; }
        }
        float v[4];
#pragma unroll
        for (int r = 0; r < 4; ++r) { const float lo = (n & 2) ? o[1][r] : o[0][r], hi = (n & 2) ? o[3][r] : o[2][r]; v[r] = (n & 4) ? hi : lo; }
        if (n < 8) stg(rs, ooff, cvtpk(v[0], v[1]), cvtpk(v[2], v[3]), T0, T1);
    }
    STAMP(5);
    if (owner) {
        const unsigned ba = L_K + (4 * g + r1) * 928 + t * 224 + c4 * 8;
        const int q4 = n >> 2, h = j >> 3;
        const int oo = WS_O + ((own * 4 + t) * 64 + lane) * 16, so = WS_S + (((kvh * 2 + h) * 4 + t) * 8 + (lane >> 3)) * 64;
        const int foff0 = WS_FIN + ((((n & 3) * 28 + 7 * t + q4) * 128 + own) * 4 + g) * 16;
        unsigned ab[4];
#pragma unroll
        for (int q = 0; q < 4; ++q) ab[q] = q4 == q ? L_A + (n & 3) * 64 + g * 16 : L_Z;
        const v4u sel = v4u{n == 0 ? 0x00003F80u : n == 1 ? 0x3F800000u : 0u, 0u, n == 2 ? 0x00003F80u : n == 3 ? 0x3F800000u : 0u, 0u};
        f32x4 acc0 = {0.f, 0.f, 0.f, 0.f}, acc1 = {0.f, 0.f, 0.f, 0.f};
        v4u wf[7], og, sg;
        float wgt = 0.f;
        bool havew = false;
        wait_prog(nW + 32 + 12);
#pragma unroll
        for (int T = 0; T < 7; ++T) wf[T] = trfrag(ba + 16 * 928 + T * 32, ba + 32 * 928 + T * 32);
        if (mlpo) {
            wait_lds_ge(F_AM, 4);
            const v2u am = lds64(L_AM + (n & 3) * 32 + g * 8);
#pragma unroll
            for (int T = 0; T < 7; ++T) {
                const v2u wl = tr64(ba + T * 32);
                const bool on = q4 == (T & 3);
                const f32x4 d = mfma(v4u{wl.x, wl.y, 0u, 0u}, v4u{on ? am.x : 0u, on ? am.y : 0u, 0u, 0u}, T < 4 ? acc0 : acc1);
                if (T < 4) acc0 = d; else acc1 = d;
            }
        }
#pragma unroll 1
        for (unsigned it = 0; it < SPIN_CAP; ++it) {
            cbar();
            og = ldg(rs, oo);
            if (!havew) {
                sg = ldg(rs, so);
                if (__all(tags_ok(sg, T0, T1))) {
                    const float m = __uint_as_float(sg.x), l = __uint_as_float(sg.z);
                    const float M = xrow_max(fmaxf(m, dpp<ROR8>(m))), e = __builtin_amdgcn_exp2f(m - M);
                    float le = l * e; le += dpp<ROR8>(le);
                    wgt = e * __builtin_amdgcn_rcpf(xrow_sum(le));
                    havew = true;
                }
            }
            if (havew && __all(tags_ok(og, T0, T1))) break;
            if (OSLEEP) __builtin_amdgcn_s_sleep(OSLEEP);
        }
        STAMP(6);
        const v4u pb = v4u{cvtpk(bflo(og.x) * wgt, bfhi(og.x) * wgt), 0u, cvtpk(bflo(og.z) * wgt, bfhi(og.z) * wgt), 0u};
        const f32x4 ds = mfma(sel, pb, f32x4{0.f, 0.f, 0.f, 0.f});
        float v[4];
#pragma unroll
        for (int r = 0; r < 4; ++r) v[r] = ds[r] + dpp<ROR8>(ds[r]);
        if (lane < 8) *(LAS v2u*)(L_A + t * 64 + (8 * (lane & 3) + 4 * (lane >> 2)) * 2) = v2u{cvtpk(v[0], v[1]), cvtpk(v[2], v[3])};
        lds_signal(F_A);
        spin_lds_ge(F_A, 4);
        v4u B[4];
#pragma unroll
        for (int q = 0; q < 4; ++q) B[q] = lds128(ab[q]);
#pragma unroll
        for (int T = 0; T < 4; ++T) { acc0 = mfma(wf[T], B[T], acc0); if (T < 3) acc1 = mfma(wf[4 + T], B[T], acc1); }
        stg(rs, foff0, cvtpk(acc0[0], acc0[1]), cvtpk(acc0[2], acc0[3]), T0, T1);
        if (q4 < 3) stg(rs, foff0 + 4 * 128 * 4 * 16, cvtpk(acc1[0], acc1[1]), cvtpk(acc1[2], acc1[3]), T0, T1);
    }
    STAMP(7);
    if (t == 3 && fin) {
        const int fo = WS_FIN + ((fb * 28 + fc) * 512 + lane) * 16;
        {
#pragma unroll 1
            for (unsigned it = 0; it < SPIN_CAP; ++it) {
                cbar();
                bool ok = true;
                if (lane < 8) ok = tags_ok(ldg(rs, WS_FIN + ((fb * 28 + fc) * 512 + (lane * 16 + PROBEJ) * 4) * 16), T0, T1);
                if (__all(ok)) break;
                __builtin_amdgcn_s_sleep(FPROBE);
            }
            if (PSLEEP) __builtin_amdgcn_s_sleep(PSLEEP);
        }
        v4u f[8];
#pragma unroll 1
        for (unsigned it = 0; it < SPIN_CAP; ++it) {
            cbar();
            bool ok = true;
#pragma unroll
            for (int i = 0; i < 8; ++i) f[i] = ldg(rs, fo + i * 1024);
#pragma unroll
            for (int i = 0; i < 8; ++i) ok &= tags_ok(f[i], T0, T1);
            if (__all(ok)) break;
        }
        const v4u sel = v4u{n == 0 ? 0x00003F80u : n == 1 ? 0x3F800000u : 0u, 0u, n == 2 ? 0x00003F80u : n == 3 ? 0x3F800000u : 0u, 0u};
        f32x4 d0 = {0.f, 0.f, 0.f, 0.f}, d1 = {0.f, 0.f, 0.f, 0.f};
#pragma unroll
        for (int i = 0; i < 8; i += 2) { d0 = mfma(sel, f[i], d0); d1 = mfma(sel, f[i + 1], d1); }
        float v[4];
#pragma unroll
        for (int r = 0; r < 4; ++r) { float x = d0[r] + d1[r]; x += dpp<ROR8>(x); v[r] = x + dpp<ROR4>(x); }
        if (lane < 4) {
            asm volatile("" : "+v"(xres));
            *(GAS v2u*)(a.out + fb * 448 + 16 * fc + 4 * lane) = v2u{cvtpk(bflo(xres.x) + v[0], bfhi(xres.x) + v[1]), cvtpk(bflo(xres.y) + v[2], bfhi(xres.y) + v[3])};
        }
        if (blk == 128 && lane == 0) *(GAS unsigned*)(a.ws + WS_E) = Eu + 1u;
        STAMP(8);
    }
    STAMP_FLUSH();
}
}

extern "C" void kernel_launch(void* const* d_in, const int* in_sizes, int n_in, void* d_out, int out_size, void* d_ws, size_t ws_size, hipStream_t stream) {
    (void)in_sizes; (void)n_in; (void)out_size; (void)ws_size;
    fk::Args a;
    a.x = (const unsigned char*)d_in[0]; a.k = (const unsigned char*)d_in[1]; a.v = (const unsigned char*)d_in[2];
    a.wq = (const unsigned char*)d_in[3]; a.wo = (const unsigned char*)d_in[4]; a.w1 = (const unsigned char*)d_in[6];
    a.b1 = (const unsigned char*)d_in[7]; a.w2 = (const unsigned char*)d_in[8]; a.out = (unsigned short*)d_out; a.ws = (unsigned char*)d_ws;
    (void)hipFuncSetAttribute((const void*)fk::fused, hipFuncAttributeMaxDynamicSharedMemorySize, fk::LDS_BYTES);
    hipLaunchKernelGGL(fk::fused, dim3(256), dim3(512), fk::LDS_BYTES, stream, a);
}
```

```cpp
#include <hip/hip_runtime.h>
#include <hip/hip_bf16.h>
#include <stdint.h>

#define GAS __attribute__((address_space(1)))
#define LAS __attribute__((address_space(3)))
typedef unsigned v4u __attribute__((ext_vector_type(4)));
typedef unsigned v2u __attribute__((ext_vector_type(2)));
typedef float f32x4 __attribute__((ext_vector_type(4)));
typedef float f32x2 __attribute__((ext_vector_type(2)));
typedef short s16x4 __attribute__((ext_vector_type(4)));
typedef __bf16 bf16x8_t __attribute__((ext_vector_type(8)));
typedef __bf16 bf16x2_t __attribute__((ext_vector_type(2)));

#define STAMPS 0
#define SETPRIO 3
#define QPOLL 14
#define FPROBE 6
#define WINK 6
#define WINSW 4
#define BS 4
#define PROBE8 1
#define PROBEJ 15
#define PSLEEP 0
#define OSLEEP 0
#define WINL 8
#define PREFETCH_CODE 2

namespace fk {
constexpr int AUX_SC1 = 16;
constexpr int L_K = 0;
constexpr int L_WQ = 67584;
constexpr int L_V = L_WQ + 16384;
constexpr int L_X = L_V + 69632 - 8192 - 4608;
constexpr int L_W1 = L_V + 69632 - 8192;
constexpr int L_M = L_V + 69632;
constexpr int L_Q = L_M;
constexpr int L_P = L_Q + 1024;
constexpr int L_A = L_P + 512;
constexpr int L_AM = L_A + 256;
constexpr int L_MX = L_AM + 128;
constexpr int L_LS = L_MX + 32;
constexpr int L_Z = L_LS + 32;
constexpr int L_F = L_Z + 16;
constexpr int F_S1 = L_F + 16, F_QK = L_F + 20, F_M = L_F + 24, F_P = L_F + 28, F_Q = L_F + 32, F_A = L_F + 36, F_AM = L_F + 40;
constexpr int L_PF = L_F + 64;
constexpr int LDS_BYTES = L_PF + 1024;
static_assert(L_X >= L_V + 52 * 1088, "X / W1 images alias V instructions >= 52 only");
static_assert(48 * 928 <= 67584, "projection rows inside the K image");
static_assert(LDS_BYTES <= 163840, "LDS");
static_assert((L_F & 15) == 0 && (L_MX & 15) == 0 && (L_A & 15) == 0 && (L_Z & 15) == 0, "alignment");
constexpr int WS_E = 0;
constexpr int WS_Q = 4096;
constexpr int WS_H = WS_Q + 4 * 8 * 4 * 128 * 16;
constexpr int WS_O = WS_H + 4 * 7 * 448 * 16;
constexpr int WS_S = WS_O + 128 * 256 * 16;
constexpr int WS_FIN = WS_S + 512 * 64;
constexpr int WS_ST = WS_FIN + 4 * 28 * 512 * 16;
constexpr int WS_END = WS_ST + 256 * 8 * 16 * 8;
constexpr unsigned SPIN_CAP = 1u << 17;

struct Args {
    const unsigned char* x; const unsigned char* k; const unsigned char* v; const unsigned char* wq; const unsigned char* wo;
    const unsigned char* w1; const unsigned char* b1; const unsigned char* w2; unsigned short* out; unsigned char* ws;
};

__device__ __forceinline__ unsigned long long rt() { return __builtin_amdgcn_s_memrealtime(); }
template <bool NT> __device__ __forceinline__ void dma16(unsigned voff, unsigned long long sbase, unsigned lds) {
    if (NT) asm volatile("s_mov_b32 m0, %2\n\ts_nop 0\n\tglobal_load_lds_dwordx4 %0, %1 nt" :: "v"(voff), "s"(sbase), "s"(lds) : "memory");
    else    asm volatile("s_mov_b32 m0, %2\n\ts_nop 0\n\tglobal_load_lds_dwordx4 %0, %1" :: "v"(voff), "s"(sbase), "s"(lds) : "memory");
}
template <int N> __device__ __forceinline__ void waitv() { asm volatile("s_waitcnt vmcnt(%0)" :: "i"(N) : "memory"); }
__device__ __forceinline__ void cbar() { asm volatile("" ::: "memory"); }

__device__ __forceinline__ f32x4 mfma(v4u a, v4u b, f32x4 c) {
    return __builtin_amdgcn_mfma_f32_16x16x32_bf16(__builtin_bit_cast(bf16x8_t, a), __builtin_bit_cast(bf16x8_t, b), c, 0, 0, 0);
}
__device__ __forceinline__ v4u lds128(unsigned a) { return *(const LAS v4u*)a; }
__device__ __forceinline__ v2u lds64(unsigned a) { return *(const LAS v2u*)a; }
__device__ __forceinline__ v2u tr64(unsigned a) { return __builtin_bit_cast(v2u, __builtin_amdgcn_ds_read_tr16_b64_v4i16((LAS s16x4*)a)); }
__device__ __forceinline__ v4u trfrag(unsigned a0, unsigned a1) { v2u lo = tr64(a0), hi = tr64(a1); return v4u{lo.x, lo.y, hi.x, hi.y}; }
__device__ __forceinline__ unsigned cvtpk(float lo, float hi) { return __builtin_bit_cast(unsigned, __builtin_convertvector(f32x2{lo, hi}, bf16x2_t)); }
__device__ __forceinline__ float bflo(unsigned u) { return __builtin_bit_cast(float, u << 16); }
__device__ __forceinline__ float bfhi(unsigned u) { return __builtin_bit_cast(float, u & 0xffff0000u); }
template <int CTRL> __device__ __forceinline__ float dpp(float x) {
    return __builtin_bit_cast(float, __builtin_amdgcn_mov_dpp(__builtin_bit_cast(int, x), CTRL, 0xf, 0xf, true));
}
constexpr int XOR1 = 0xB1, XOR2 = 0x4E, HMIR = 0x141, ROR4 = 0x124, ROR8 = 0x128, NBC4 = 0x154;
__device__ __forceinline__ float fmx(float a, float b) { float r; asm("v_max_f32 %0, %1, %2" : "=v"(r) : "v"(a), "v"(b)); return r; }
__device__ __forceinline__ float xrow_max(float x) {
    auto s = __builtin_amdgcn_permlane16_swap(__float_as_uint(x), __float_as_uint(x), false, false);
    x = fmx(__uint_as_float(s[0]), __uint_as_float(s[1]));
    auto t = __builtin_amdgcn_permlane32_swap(__float_as_uint(x), __float_as_uint(x), false, false);
    return fmx(__uint_as_float(t[0]), __uint_as_float(t[1]));
}
__device__ __forceinline__ float x32_sum(float x) {
    auto t = __builtin_amdgcn_permlane32_swap(__float_as_uint(x), __float_as_uint(x), false, false);
    return __uint_as_float(t[0]) + __uint_as_float(t[1]);
}
__device__ __forceinline__ float x16_sum(float x) {
    auto s = __builtin_amdgcn_permlane16_swap(__float_as_uint(x), __float_as_uint(x), false, false);
    return __uint_as_float(s[0]) + __uint_as_float(s[1]);
}
__device__ __forceinline__ float xrow_sum(float x) {
    auto s = __builtin_amdgcn_permlane16_swap(__float_as_uint(x), __float_as_uint(x), false, false);
    return x32_sum(__uint_as_float(s[0]) + __uint_as_float(s[1]));
}
__device__ __forceinline__ float par16_sum(float x) { x += dpp<XOR2>(x); x += dpp<ROR4>(x); return x + dpp<ROR8>(x); }
__device__ __forceinline__ float bperm(int srclane, float v) { return __builtin_bit_cast(float, __builtin_amdgcn_ds_bpermute(srclane * 4, __builtin_bit_cast(int, v))); }

__device__ __forceinline__ void wait_lds_ge(unsigned addr, unsigned want) {
#pragma unroll 1
    for (unsigned i = 0; i < (1u << 22); ++i) { if ((unsigned)__builtin_amdgcn_readfirstlane(*(volatile LAS unsigned*)addr) >= want) break; __builtin_amdgcn_s_sleep(1); }
    cbar();
}
__device__ __forceinline__ void spin_lds_ge(unsigned addr, unsigned want) {
#pragma unroll 1
    for (unsigned i = 0; i < (1u << 24); ++i) { if ((unsigned)__builtin_amdgcn_readfirstlane(*(volatile LAS unsigned*)addr) >= want) break; }
    cbar();
}
__device__ __forceinline__ void wait_prog(unsigned want) {
#pragma unroll 1
    for (unsigned i = 0; i < (1u << 22); ++i) {
        const v4u p = *(volatile LAS v4u*)L_F;
        const unsigned m01 = p.x < p.y ? p.x : p.y, m23 = p.z < p.w ? p.z : p.w;
        if ((unsigned)__builtin_amdgcn_readfirstlane(m01 < m23 ? m01 : m23) >= want) break;
        __builtin_amdgcn_s_sleep(1);
    }
    cbar();
}
__device__ __forceinline__ void wait_prog_c(unsigned want, unsigned& seen) {
#pragma unroll 1
    for (unsigned i = 0; i < (1u << 24) && seen < want; ++i) {
        const v4u p = *(volatile LAS v4u*)L_F;
        const unsigned m01 = p.x < p.y ? p.x : p.y, m23 = p.z < p.w ? p.z : p.w;
        seen = (unsigned)__builtin_amdgcn_readfirstlane(m01 < m23 ? m01 : m23);
    }
    cbar();
}
__device__ __forceinline__ void lds_signal(unsigned addr) {
    asm volatile("s_waitcnt lgkmcnt(0)\n\ts_mov_b64 exec, 1\n\tds_add_u32 %0, %1\n\ts_mov_b64 exec, -1" :: "v"(addr), "v"(1u) : "memory");
}
__device__ __forceinline__ bool tags_ok(v4u g, unsigned t0, unsigned t1) { return (g.y == t0) & (g.w == t1); }
__device__ __forceinline__ v4u ldg(__amdgpu_buffer_rsrc_t rs, int off) { return __builtin_amdgcn_raw_buffer_load_b128(rs, off, 0, AUX_SC1); }
__device__ __forceinline__ void stg(__amdgpu_buffer_rsrc_t rs, int off, unsigned p0, unsigned p1, unsigned t0, unsigned t1) { __builtin_amdgcn_raw_buffer_store_b128(v4u{p0, t0, p1, t1}, rs, off, 0, AUX_SC1); }

#define STAMP(i) do { } while (0)
#define NPOLL(i, v) do { } while (0)
#define STAMP_DECL do { } while (0)
#define STAMP_FLUSH() do { } while (0)

__global__ void __launch_bounds__(512, 2) fused(Args a) {
    const int tid = threadIdx.x, lane = tid & 63, w = __builtin_amdgcn_readfirstlane(tid >> 6);
    const int blk = blockIdx.x, kvh = blk & 7, j = blk >> 3, b = j >> 3, s = j & 7;
    const bool hrole = blk < 196, owner = j < 16;
    const int own = kvh * 16 + j;
    const bool mlpo = owner && own < 112;
    const int nq = b < 2 ? 4 : 3;
    const unsigned nW = 1 + nq + (hrole ? 2 : 0);
    STAMP_DECL;
    STAMP(0);
    {
        unsigned long long pc;
        asm volatile("s_getpc_b64 %0" : "=s"(pc));
        pc = (pc & ~0xFFull) + w * 1536;
        dma16<false>(lane * 16, pc, L_PF);
        dma16<false>(lane * 16, pc + 1024, L_PF);
    }
    if (__builtin_expect(w < 4, 1)) {
        const int L = w;
        if (lane < 56) dma16<false>(lane * 16, (unsigned long long)a.x + L * 896, L_X + L * 912);
        const unsigned f = ((lane >> 4) & 1) | ((L & 1) << 1), sp = lane & 7, gp = (((sp >> 1) ^ f) << 1) | (sp & 1);
        {
            const int r0 = b < 2 ? b * 128 : 256 + (b - 2) * 96;
            unsigned long long p = (unsigned long long)a.wq + (size_t)(r0 + 8 * L) * 8192 + (kvh * 512 + s * 64) * 2; unsigned l = L_WQ + L * 1024;
            const unsigned voff = (lane >> 3) * 8192 + gp * 16;
#pragma unroll 1
            for (int i = 0; i < nq; ++i) { dma16<true>(voff, p, l); p += 32 * 8192; l += 4096; }
        }
        if (hrole) {
            const int cb = blk % 28, rb = blk / 28;
            unsigned long long p = (unsigned long long)a.w1 + (size_t)(rb * 64 + 8 * L) * 3584 + cb * 128;
            const unsigned voff = (lane >> 3) * 3584 + gp * 16;
            dma16<true>(voff, p, L_W1 + L * 1024);
            dma16<true>(voff, p + 32 * 3584, L_W1 + L * 1024 + 4096);
        }
        const size_t kvbase = ((size_t)(b * 8 + kvh) * 4096 + 3072) * 512 + (size_t)s * (BS * 1024) + L * 1024;
#define KVSTEP(i) ((((i) + 1) * 4 % BS == 0) ? (7 * BS + 4) * 1024 : 4096)
        unsigned n = nW;
        volatile LAS unsigned* prog = (volatile LAS unsigned*)(L_F + 4 * L);
        {
            const unsigned voff = (lane >> 5) * 512 + ((lane & 31) ^ (lane >> 5)) * 16;
            unsigned long long p = (unsigned long long)a.k + kvbase; unsigned l = L_K + L * 1056;
            dma16<true>(voff, p, l); ++n; p += KVSTEP(0); l += 4224;
            __builtin_amdgcn_s_barrier();
#pragma unroll 1
            for (int i = 1; i < 16; ++i) { dma16<true>(voff, p, l); ++n; waitv<WINK - 1>(); const int d = (int)n - (WINK - 1); *prog = d > 0 ? d : 0; p += KVSTEP(i); l += 4224; }
        }
        STAMP(1);
        {
            const unsigned voff = (lane >> 5) * 512 + ((lane & 31) ^ ((lane >> 5) << 1)) * 16;
            unsigned long long p = (unsigned long long)a.v + kvbase; unsigned l = L_V + L * 1088;
#pragma unroll 1
            for (int i = 0; i < 16; ++i) {
                if (i == 12) wait_lds_ge(F_S1, 4);
                dma16<true>(voff, p, l); ++n;
                if (i < WINSW) { waitv<WINK - 1>(); *prog = n - (WINK - 1); } else { waitv<WINL - 1>(); *prog = n - (WINL - 1); }
                p += KVSTEP(i); l += 4352;
            }
        }
        STAMP(2);
        if (owner) {
            wait_lds_ge(F_P, 4);
            const unsigned voff = lane * 16;
            unsigned l = L_K + L * 928;
            const unsigned long long w2p = (unsigned long long)a.w2 + (unsigned)(((mlpo ? own : 0) * 16 + L) * 896);
            const unsigned long long wop = (unsigned long long)a.wo + (unsigned)((own * 32 + L) * 896);
            if (lane < 56) {
#pragma unroll
                for (int i = 0; i < 4; ++i) dma16<true>(voff, w2p + i * 4 * 896, l + i * 3712);
            }
            waitv<8>(); *prog = n - 4;
            waitv<4>(); *prog = n;
            STAMP(4);
            if (lane < 56) {
#pragma unroll
                for (int i = 0; i < 8; ++i) dma16<true>(voff, wop + i * 4 * 896, l + (4 + i) * 3712);
            }
            waitv<8>(); *prog = n + 4;
            waitv<0>(); *prog = n + 12;
        } else {
            waitv<4>(); *prog = n - 4;
            waitv<0>(); *prog = n;
            STAMP(4);
        }
        STAMP(3);
        STAMP_FLUSH();
        return;
    }
    const int t = w - 4, n = lane & 15, g = lane >> 4;
    __builtin_amdgcn_s_setprio(SETPRIO);
    if (t == 0) {
        if (lane < 20) *(LAS unsigned*)(L_Z + 4 * lane) = 0u;
        if (lane < 56) *(LAS v4u*)(L_X + 4 * 912 + lane * 16) = v4u{0x3F803F80u, 0x3F803F80u, 0x3F803F80u, 0x3F803F80u};
    }
    asm volatile("s_waitcnt lgkmcnt(0)" ::: "memory");
    __builtin_amdgcn_s_barrier();
    cbar();
    const __amdgpu_buffer_rsrc_t rs = __builtin_amdgcn_make_buffer_rsrc(a.ws, 0, WS_END, 0x00020000);
    const unsigned Eu = *(const __attribute__((address_space(4))) unsigned*)(a.ws + WS_E);
    const unsigned T0 = (Eu * 0x9E3779B1u + 0x7F4A7C15u) & 0xBFFFBFFFu, T1 = ((Eu ^ 0x5BD1E995u) * 0x85EBCA6Bu + 0x165667B1u) & 0xBFFFBFFFu;
    v2u b1v = v2u{0u, 0u};
    if (mlpo && lane < 32) b1v = *(const GAS v2u*)(a.b1 + (own * 16 + (lane & 3) * 4) * 2);
    const int fi = blk - 128, fb = fi / 28, fc = fi % 28;
    const bool fin = fi >= 0 && fi < 112;
    v2u xres = v2u{0u, 0u};
    if (t == 3 && fin && lane < 4) xres = *(const GAS v2u*)(a.x + (fb * 448 + 16 * fc + 4 * lane) * 2);
    const unsigned r1 = n >> 2, c4 = n & 3;

    {
        const unsigned xa = L_X + (n < 4 ? n : 4) * 912 + g * 16;
        const unsigned fs = ((r1 >> 1) & 1) | ((g & 1) << 1);
        const unsigned wo_ = (8 * g + r1) * 128 + ((t ^ fs) * 32) + c4 * 8;
        const int ks0 = b < 2 ? b * 4 : 8 + (b - 2) * 3, rb = blk / 28;
        const int qoff = WS_Q + (((n * 8 + kvh) * 4 + b) * 128 + s * 16 + t * 4 + g) * 16;
        const int hoff = WS_H + ((n * 7 + rb) * 448 + (blk % 28) * 16 + t * 4 + g) * 16;
        wait_prog(1);
        v4u xf[14];
#pragma unroll
        for (int ks = 0; ks < 14; ++ks) xf[ks] = lds128(xa + 64 * ks);
        v4u bq[4], bh[2];
#pragma unroll
        for (int kk = 0; kk < 4; ++kk) bq[kk] = lds128(xa + 64 * (ks0 + (kk < nq ? kk : 0)));
#pragma unroll
        for (int kk = 0; kk < 2; ++kk) bh[kk] = lds128(xa + 64 * ((hrole ? 2 * rb : 0) + kk));
        __builtin_amdgcn_sched_barrier(0);
        f32x4 G0 = {0.f, 0.f, 0.f, 0.f}, G1 = {0.f, 0.f, 0.f, 0.f};
#pragma unroll
        for (int ks = 0; ks < 14; ks += 2) { G0 = mfma(xf[ks], xf[ks], G0); G1 = mfma(xf[ks + 1], xf[ks + 1], G1); }
        const f32x4 G = G0 + G1;
        const float gsel = n == 0 ? G[0] : n == 1 ? G[1] : n == 2 ? G[2] : G[3];
        const float sq = bperm(n & 3, gsel), sm = bperm(16 + (n & 3), G[0]);
        const float mean = sm * (1.0f / 448.0f);
        const float var = fmaxf(fmaf(sq, 1.0f / 448.0f, -mean * mean), 0.f);
        const float rstd = __builtin_amdgcn_rsqf(var + 1e-5f), mr = mean * rstd;
        asm volatile("" :: "v"(rstd), "v"(mr), "v"(qoff), "v"(hoff));
        wait_prog(nW);
        STAMP(1);
        v4u aq[4], ah[2];
#pragma unroll
        for (int kk = 0; kk < 4; ++kk) aq[kk] = trfrag(L_WQ + wo_ + kk * 4096, L_WQ + wo_ + kk * 4096 + 512);
#pragma unroll
        for (int kk = 0; kk < 2; ++kk) ah[kk] = trfrag(L_W1 + wo_ + kk * 4096, L_W1 + wo_ + kk * 4096 + 512);
        __builtin_amdgcn_sched_barrier(0);
        f32x4 Dq = {0.f, 0.f, 0.f, 0.f}, Dq2 = {0.f, 0.f, 0.f, 0.f}, Dh = {0.f, 0.f, 0.f, 0.f};
        Dq = mfma(aq[0], bq[0], Dq);
        Dq2 = mfma(aq[1], bq[1], Dq2);
        Dh = mfma(ah[0], bh[0], Dh);
        Dq = mfma(aq[2], bq[2], Dq);
        if (nq == 4) Dq2 = mfma(aq[3], bq[3], Dq2);
        Dh = mfma(ah[1], bh[1], Dh);
        Dq += Dq2;
        lds_signal(F_S1);
        float q[4], h[4];
#pragma unroll
        for (int r = 0; r < 4; ++r) { q[r] = fmaf(rstd, Dq[r], -mr * dpp<NBC4>(Dq[r])); h[r] = fmaf(rstd, Dh[r], -mr * dpp<NBC4>(Dh[r])); }
        if (n < 4) {
            stg(rs, qoff, cvtpk(q[0], q[1]), cvtpk(q[2], q[3]), T0, T1);
            if (hrole) stg(rs, hoff, cvtpk(h[0], h[1]), cvtpk(h[2], h[3]), T0, T1);
        }
    }
    STAMP(2);
    const int gid = b * 8 + kvh;
    v4u hx = v4u{0u, 0u, 0u, 0u};
    const bool hval = mlpo && lane < 28;
    {
        const int qo = WS_Q + (gid * 512 + (lane >> 5) * 128 + 32 * t + (lane & 31)) * 16;
        const int ho = hval ? WS_H + ((t * 7 + (lane >> 2)) * 448 + own * 4 + (lane & 3)) * 16 : qo;
        v4u x0, x1;
        wait_prog(nW + QPOLL);
#pragma unroll 1
        for (unsigned it = 0; it < SPIN_CAP; ++it) {
            cbar();
            NPOLL(9, 1);
            x0 = ldg(rs, qo); x1 = ldg(rs, qo + 256 * 16); hx = ldg(rs, ho);
            if (__all(tags_ok(x0, T0, T1) & tags_ok(x1, T0, T1) & (!hval | tags_ok(hx, T0, T1)))) break;
        }
        STAMP(3);
        const float q0 = x32_sum(bflo(x0.x) + bflo(x1.x)), q1 = x32_sum(bfhi(x0.x) + bfhi(x1.x));
        const float q2 = x32_sum(bflo(x0.z) + bflo(x1.z)), q3 = x32_sum(bfhi(x0.z) + bfhi(x1.z));
        if (lane < 32) *(LAS v2u*)(L_Q + (32 * t + lane) * 8) = v2u{cvtpk(q0, q1), cvtpk(q2, q3)};
        lds_signal(F_Q);
    }
    {
        const unsigned ka = L_K + t * 16896 + (n >> 1) * 1056 + (n & 1) * 512 + ((g ^ (n & 1)) * 16);
        wait_prog(nW + 4 * (t + 1));
        v4u kf[16];
#pragma unroll
        for (int i = 0; i < 16; ++i) kf[i] = lds128(ka + (i >> 3) * 8448 + (i & 7) * 64);
        spin_lds_ge(F_Q, 4);
        v4u qf[8];
#pragma unroll
        for (int ks = 0; ks < 8; ++ks) qf[ks] = lds128(L_Q + (n & 1) * 512 + ks * 64 + g * 16);
        __builtin_amdgcn_sched_barrier(0);
        f32x4 z0 = {0.f, 0.f, 0.f, 0.f}, z1 = {0.f, 0.f, 0.f, 0.f};
#pragma unroll
        for (int ks = 0; ks < 8; ++ks) { z0 = mfma(kf[ks], qf[ks], z0); z1 = mfma(kf[8 + ks], qf[ks], z1); }
        constexpr float C = 0.0625f * 1.4426950408889634f;
        const float z[4] = {(n & 2) ? z1[0] : z0[0], (n & 2) ? z1[1] : z0[1], (n & 2) ? z1[2] : z0[2], (n & 2) ? z1[3] : z0[3]};
        float m = fmx(fmx(z[0], z[1]), fmx(z[2], z[3])) * C;
        m = xrow_max(fmx(m, dpp<XOR2>(m)));
        float p[4];
#pragma unroll
        for (int r = 0; r < 4; ++r) p[r] = __builtin_amdgcn_exp2f(fmaf(z[r], C, -m));
        if (n < 4) *(LAS v2u*)(L_P + (n & 1) * 256 + (32 * t + 8 * g + 4 * (n >> 1)) * 2) = v2u{cvtpk(p[0], p[1]), cvtpk(p[2], p[3])};
        float l = (p[0] + p[1]) + (p[2] + p[3]);
        l = xrow_sum(l + dpp<XOR2>(l));
        if (lane < 2) { *(LAS float*)(L_MX + lane * 16 + t * 4) = m; *(LAS float*)(L_LS + lane * 16 + t * 4) = l; }
        lds_signal(F_P);
    }
    STAMP(4);
    if (mlpo) {
        const float hv[4] = {hval ? bflo(hx.x) : 0.f, hval ? bfhi(hx.x) : 0.f, hval ? bflo(hx.z) : 0.f, hval ? bfhi(hx.z) : 0.f};
        const float bb[4] = {bflo(b1v.x), bfhi(b1v.x), bflo(b1v.y), bfhi(b1v.y)};
        float act[4];
#pragma unroll
        for (int r = 0; r < 4; ++r) {
            float v = hv[r]; v += dpp<ROR8>(v); v += dpp<ROR4>(v); v = x16_sum(v) + bb[r];
            const float e = __builtin_amdgcn_exp2f(-1.4426950408889634f * fabsf(v));
            const float num = v > 0.f ? fmaf(2.f, e, 1.f) : e * (e + 2.f);
            const float den = v > 0.f ? fmaf(2.f * e, e, num) : num + 2.f;
            act[r] = v * num * __builtin_amdgcn_rcpf(den);
        }
        if (lane < 4) *(LAS v2u*)(L_AM + t * 32 + lane * 8) = v2u{cvtpk(act[0], act[1]), cvtpk(act[2], act[3])};
        lds_signal(F_AM);
    }
    {
        const unsigned e = r1 & 1;
        const unsigned vb = L_V + (2 * g + (r1 >> 1)) * 1088 + e * 512 + t * 128 + c4 * 8;
        const unsigned va0 = vb + e * 32, va1 = vb + (e ^ 1) * 32;
        const int ooff = WS_O + ((((kvh * 16 + (n & 1) * 8 + 2 * t + ((n >> 2) & 1)) * 4 + b) * 8 + s) * 8 + 4 * ((n >> 1) & 1) + g) * 16;
        const int soff = WS_S + ((((kvh * 2 + (lane & 1)) * 4 + b) * 8 + s) * 4 + ((lane >> 1) & 3)) * 16;
        int ooffp = ooff, soffp = soff;
        asm volatile("" : "+v"(ooffp), "+v"(soffp));
        spin_lds_ge(F_P, 4);
        v4u pf[4];
#pragma unroll
        for (int kk = 0; kk < 4; ++kk) pf[kk] = lds128(L_P + (n & 1) * 256 + kk * 64 + g * 16);
        const v4u mx = lds128(L_MX + (n & 1) * 16), ls = lds128(L_LS + (n & 1) * 16);
        const float mk[4] = {__uint_as_float(mx.x), __uint_as_float(mx.y), __uint_as_float(mx.z), __uint_as_float(mx.w)};
        const float mW = fmx(fmx(mk[0], mk[1]), fmx(mk[2], mk[3]));
        float fk[4];
#pragma unroll
        for (int kk = 0; kk < 4; ++kk) fk[kk] = __builtin_amdgcn_exp2f(mk[kk] - mW);
        const float lW = fmaf(fk[0], __uint_as_float(ls.x), fk[1] * __uint_as_float(ls.y)) + fmaf(fk[2], __uint_as_float(ls.z), fk[3] * __uint_as_float(ls.w));
        if (t == 0 && lane < 8) stg(rs, soffp, __float_as_uint(mW), __float_as_uint(lW), T0, T1);
        f32x4 o[4];
        unsigned seen = 0;
        v4u vf[2][4];
        wait_prog_c(nW + 20, seen);
#pragma unroll
        for (int aa = 0; aa < 4; ++aa) { const unsigned ad = ((aa & 1) ? va1 : va0) + (aa >> 1) * 64; vf[0][aa] = trfrag(ad, ad + 8704); }
#pragma unroll
        for (int kk = 0; kk < 4; ++kk) {
            if (kk < 3) {
                wait_prog_c(nW + 16 + 4 * (kk + 2), seen);
#pragma unroll
                for (int aa = 0; aa < 4; ++aa) { const unsigned ad = ((aa & 1) ? va1 : va0) + (aa >> 1) * 64 + (kk + 1) * 17408; vf[(kk + 1) & 1][aa] = trfrag(ad, ad + 8704); }
            }
#pragma unroll
            for (int aa = 0; aa < 4; ++aa) { const f32x4 d = mfma(vf[kk & 1][aa], pf[kk], f32x4{0.f, 0.f, 0.f, 0.f}); o[aa] = kk == 0 ? fk[0] * d : o[aa] + fk[kk] * d; }
            asm volatile("" : "+v"(o[0]), "+v"(o[1]), "+v"(o[2]), "+v"(o[3]));
        }
        float v[4];
#pragma unroll
        for (int r = 0; r < 4; ++r) { const float lo = (n & 2) ? o[1][r] : o[0][r], hi = (n & 2) ? o[3][r] : o[2][r]; v[r] = (n & 4) ? hi : lo; }
        if (n < 8) stg(rs, ooffp, cvtpk(v[0], v[1]), cvtpk(v[2], v[3]), T0, T1);
    }
    STAMP(5);
    if (owner) {
        const unsigned ba = L_K + (4 * g + r1) * 928 + t * 224 + c4 * 8;
        const int q4 = n >> 2, h = j >> 3;
        const int oo = WS_O + ((own * 4 + t) * 64 + lane) * 16, so = WS_S + (((kvh * 2 + h) * 4 + t) * 8 + (lane >> 3)) * 64;
        const int foff0 = WS_FIN + ((((n & 3) * 28 + 7 * t + q4) * 128 + own) * 4 + g) * 16;
        unsigned ab[4];
#pragma unroll
        for (int q = 0; q < 4; ++q) ab[q] = q4 == q ? L_A + (n & 3) * 64 + g * 16 : L_Z;
        const v4u sel = v4u{n == 0 ? 0x00003F80u : n == 1 ? 0x3F800000u : 0u, 0u, n == 2 ? 0x00003F80u : n == 3 ? 0x3F800000u : 0u, 0u};
        v4u selp = sel;
        int foffp = foff0;
        unsigned aw = L_A + t * 64 + (8 * (lane & 3) + 4 * (lane >> 2)) * 2;
        asm volatile("" : "+v"(selp), "+v"(foffp), "+v"(aw), "+v"(ab[0]), "+v"(ab[1]), "+v"(ab[2]), "+v"(ab[3]));
        f32x4 acc0 = {0.f, 0.f, 0.f, 0.f}, acc1 = {0.f, 0.f, 0.f, 0.f};
        v4u wf[7], og, sg;
        float wgt = 0.f;
        wait_prog(nW + 32 + 4);
        if (mlpo) {
            wait_lds_ge(F_AM, 4);
            const v2u am = lds64(L_AM + (n & 3) * 32 + g * 8);
#pragma unroll
            for (int T = 0; T < 7; ++T) {
                const v2u wl = tr64(ba + T * 32);
                const bool on = q4 == (T & 3);
                const f32x4 d = mfma(v4u{wl.x, wl.y, 0u, 0u}, v4u{on ? am.x : 0u, on ? am.y : 0u, 0u, 0u}, T < 4 ? acc0 : acc1);
                if (T < 4) acc0 = d; else acc1 = d;
            }
        }
        wait_prog(nW + 32 + 12);
#pragma unroll
        for (int T = 0; T < 7; ++T) wf[T] = trfrag(ba + 16 * 928 + T * 32, ba + 32 * 928 + T * 32);
        bool havew = false;
#pragma unroll 1
        for (unsigned it = 0; it < SPIN_CAP; ++it) {
            cbar();
            og = ldg(rs, oo);
            if (!havew) {
                sg = ldg(rs, so);
                if (__all(tags_ok(sg, T0, T1))) {
                    const float m = __uint_as_float(sg.x), l = __uint_as_float(sg.z);
                    const float M = xrow_max(fmx(m, dpp<ROR8>(m))), e = __builtin_amdgcn_exp2f(m - M);
                    float le = l * e; le += dpp<ROR8>(le);
                    wgt = e * __builtin_amdgcn_rcpf(xrow_sum(le));
                    havew = true;
                }
            }
            if (havew && __all(tags_ok(og, T0, T1))) break;
            if (OSLEEP) __builtin_amdgcn_s_sleep(OSLEEP);
        }
        STAMP(6);
        const v4u pb = v4u{cvtpk(bflo(og.x) * wgt, bfhi(og.x) * wgt), 0u, cvtpk(bflo(og.z) * wgt, bfhi(og.z) * wgt), 0u};
        const f32x4 ds = mfma(selp, pb, f32x4{0.f, 0.f, 0.f, 0.f});
        float v[4];
#pragma unroll
        for (int r = 0; r < 4; ++r) v[r] = ds[r] + dpp<ROR8>(ds[r]);
        if (lane < 8) *(LAS v2u*)aw = v2u{cvtpk(v[0], v[1]), cvtpk(v[2], v[3])};
        lds_signal(F_A);
        spin_lds_ge(F_A, 4);
        v4u B[4];
#pragma unroll
        for (int q = 0; q < 4; ++q) B[q] = lds128(ab[q]);
        __builtin_amdgcn_sched_barrier(0);
#pragma unroll
        for (int T = 0; T < 4; ++T) { acc0 = mfma(wf[T], B[T], acc0); if (T < 3) acc1 = mfma(wf[4 + T], B[T], acc1); }
        stg(rs, foffp, cvtpk(acc0[0], acc0[1]), cvtpk(acc0[2], acc0[3]), T0, T1);
        if (q4 < 3) stg(rs, foffp + 4 * 128 * 4 * 16, cvtpk(acc1[0], acc1[1]), cvtpk(acc1[2], acc1[3]), T0, T1);
    }
    STAMP(7);
    if (t == 3 && fin) {
        const int fo = WS_FIN + ((fb * 28 + fc) * 512 + lane) * 16;
        v4u sel = v4u{n == 0 ? 0x00003F80u : n == 1 ? 0x3F800000u : 0u, 0u, n == 2 ? 0x00003F80u : n == 3 ? 0x3F800000u : 0u, 0u};
        float xr[4] = {bflo(xres.x), bfhi(xres.x), bflo(xres.y), bfhi(xres.y)};
        unsigned long long op = (unsigned long long)(a.out + fb * 448 + 16 * fc + 4 * (lane & 3));
        asm volatile("" : "+v"(sel), "+v"(xr[0]), "+v"(xr[1]), "+v"(xr[2]), "+v"(xr[3]), "+v"(op));
        {
#pragma unroll 1
            for (unsigned it = 0; it < SPIN_CAP; ++it) {
                cbar();
                bool ok = true;
                if (lane < 8) ok = tags_ok(ldg(rs, WS_FIN + ((fb * 28 + fc) * 512 + (lane * 16 + PROBEJ) * 4) * 16), T0, T1);
                if (__all(ok)) break;
                __builtin_amdgcn_s_sleep(FPROBE);
            }
            if (PSLEEP) __builtin_amdgcn_s_sleep(PSLEEP);
        }
        v4u f[8];
#pragma unroll 1
        for (unsigned it = 0; it < SPIN_CAP; ++it) {
            cbar();
            bool ok = true;
#pragma unroll
            for (int i = 0; i < 8; ++i) f[i] = ldg(rs, fo + i * 1024);
#pragma unroll
            for (int i = 0; i < 8; ++i) ok &= tags_ok(f[i], T0, T1);
            if (__all(ok)) break;
        }
        f32x4 d0 = {0.f, 0.f, 0.f, 0.f}, d1 = {0.f, 0.f, 0.f, 0.f};
#pragma unroll
        for (int i = 0; i < 8; i += 2) { d0 = mfma(sel, f[i], d0); d1 = mfma(sel, f[i + 1], d1); }
        float v[4];
#pragma unroll
        for (int r = 0; r < 4; ++r) { float x = d0[r] + d1[r]; x += dpp<ROR8>(x); v[r] = x + dpp<ROR4>(x); }
        if (lane < 4) {
            *(GAS v2u*)op = v2u{cvtpk(xr[0] + v[0], xr[1] + v[1]), cvtpk(xr[2] + v[2], xr[3] + v[3])};
        }
        if (blk == 128 && lane == 0) *(GAS unsigned*)(a.ws + WS_E) = Eu + 1u;
        STAMP(8);
    }
    STAMP_FLUSH();
}
}

extern "C" void kernel_launch(void* const* d_in, const int* in_sizes, int n_in, void* d_out, int out_size, void* d_ws, size_t ws_size, hipStream_t stream) {
    (void)in_sizes; (void)n_in; (void)out_size; (void)ws_size;
    fk::Args a;
    a.x = (const unsigned char*)d_in[0]; a.k = (const unsigned char*)d_in[1]; a.v = (const unsigned char*)d_in[2];
    a.wq = (const unsigned char*)d_in[3]; a.wo = (const unsigned char*)d_in[4]; a.w1 = (const unsigned char*)d_in[6];
    a.b1 = (const unsigned char*)d_in[7]; a.w2 = (const unsigned char*)d_in[8]; a.out = (unsigned short*)d_out; a.ws = (unsigned char*)d_ws;
    (void)hipFuncSetAttribute((const void*)fk::fused, hipFuncAttributeMaxDynamicSharedMemorySize, fk::LDS_BYTES);
    hipLaunchKernelGGL(fk::fused, dim3(256), dim3(512), fk::LDS_BYTES, stream, a);
}
```

```cpp
#include <hip/hip_runtime.h>
#include <hip/hip_bf16.h>
#include <stdint.h>

#define GAS __attribute__((address_space(1)))
#define LAS __attribute__((address_space(3)))
typedef unsigned v4u __attribute__((ext_vector_type(4)));
typedef unsigned v2u __attribute__((ext_vector_type(2)));
typedef float f32x4 __attribute__((ext_vector_type(4)));
typedef float f32x2 __attribute__((ext_vector_type(2)));
typedef short s16x4 __attribute__((ext_vector_type(4)));
typedef __bf16 bf16x8_t __attribute__((ext_vector_type(8)));
typedef __bf16 bf16x2_t __attribute__((ext_vector_type(2)));

#define STAMPS 0
#define SETPRIO 3
#define QPOLL 13
#define FPROBE 6
#define WINK 6
#define WINSW 4
#define BS 4
#define PROBE8 1
#define PROBEJ 15
#define PSLEEP 0
#define OSLEEP 0
#define WINL 8
#define PREFETCH_CODE 2

namespace fk {
constexpr int AUX_SC1 = 16;
constexpr int L_K = 0;
constexpr int L_WQ = 67584;
constexpr int L_V = L_WQ + 16384;
constexpr int L_X = L_V + 69632 - 8192 - 4608;
constexpr int L_W1 = L_V + 69632 - 8192;
constexpr int L_M = L_V + 69632;
constexpr int L_Q = L_M;
constexpr int L_P = L_Q + 1024;
constexpr int L_A = L_P + 512;
constexpr int L_AM = L_A + 256;
constexpr int L_MX = L_AM + 128;
constexpr int L_LS = L_MX + 32;
constexpr int L_Z = L_LS + 32;
constexpr int L_F = L_Z + 16;
constexpr int F_S1 = L_F + 16, F_QK = L_F + 20, F_M = L_F + 24, F_P = L_F + 28, F_Q = L_F + 32, F_A = L_F + 36, F_AM = L_F + 40;
constexpr int L_PF = L_F + 64;
constexpr int LDS_BYTES = L_PF + 1024;
static_assert(L_X >= L_V + 52 * 1088, "X / W1 images alias V instructions >= 52 only");
static_assert(48 * 928 <= 67584, "projection rows inside the K image");
static_assert(LDS_BYTES <= 163840, "LDS");
static_assert((L_F & 15) == 0 && (L_MX & 15) == 0 && (L_A & 15) == 0 && (L_Z & 15) == 0, "alignment");
constexpr int WS_E = 0;
constexpr int WS_Q = 4096;
constexpr int WS_H = WS_Q + 4 * 8 * 4 * 128 * 16;
constexpr int WS_O = WS_H + 4 * 7 * 448 * 16;
constexpr int WS_S = WS_O + 128 * 256 * 16;
constexpr int WS_FIN = WS_S + 512 * 64;
constexpr int WS_ST = WS_FIN + 4 * 28 * 512 * 16;
constexpr int WS_END = WS_ST + 256 * 8 * 16 * 8;
constexpr unsigned SPIN_CAP = 1u << 17;

struct Args {
    const unsigned char* x; const unsigned char* k; const unsigned char* v; const unsigned char* wq; const unsigned char* wo;
    const unsigned char* w1; const unsigned char* b1; const unsigned char* w2; unsigned short* out; unsigned char* ws;
};

__device__ __forceinline__ unsigned long long rt() { return __builtin_amdgcn_s_memrealtime(); }
template <bool NT> __device__ __forceinline__ void dma16(unsigned voff, unsigned long long sbase, unsigned lds) {
    if (NT) asm volatile("s_mov_b32 m0, %2\n\ts_nop 0\n\tglobal_load_lds_dwordx4 %0, %1 nt" :: "v"(voff), "s"(sbase), "s"(lds) : "memory");
    else    asm volatile("s_mov_b32 m0, %2\n\ts_nop 0\n\tglobal_load_lds_dwordx4 %0, %1" :: "v"(voff), "s"(sbase), "s"(lds) : "memory");
}
template <int N> __device__ __forceinline__ void waitv() { asm volatile("s_waitcnt vmcnt(%0)" :: "i"(N) : "memory"); }
__device__ __forceinline__ void cbar() { asm volatile("" ::: "memory"); }

__device__ __forceinline__ f32x4 mfma(v4u a, v4u b, f32x4 c) {
    return __builtin_amdgcn_mfma_f32_16x16x32_bf16(__builtin_bit_cast(bf16x8_t, a), __builtin_bit_cast(bf16x8_t, b), c, 0, 0, 0);
}
__device__ __forceinline__ v4u lds128(unsigned a) { return *(const LAS v4u*)a; }
__device__ __forceinline__ v2u lds64(unsigned a) { return *(const LAS v2u*)a; }
__device__ __forceinline__ v2u tr64(unsigned a) { return __builtin_bit_cast(v2u, __builtin_amdgcn_ds_read_tr16_b64_v4i16((LAS s16x4*)a)); }
__device__ __forceinline__ v4u trfrag(unsigned a0, unsigned a1) { v2u lo = tr64(a0), hi = tr64(a1); return v4u{lo.x, lo.y, hi.x, hi.y}; }
__device__ __forceinline__ unsigned cvtpk(float lo, float hi) { return __builtin_bit_cast(unsigned, __builtin_convertvector(f32x2{lo, hi}, bf16x2_t)); }
__device__ __forceinline__ float bflo(unsigned u) { return __builtin_bit_cast(float, u << 16); }
__device__ __forceinline__ float bfhi(unsigned u) { return __builtin_bit_cast(float, u & 0xffff0000u); }
template <int CTRL> __device__ __forceinline__ float dpp(float x) {
    return __builtin_bit_cast(float, __builtin_amdgcn_mov_dpp(__builtin_bit_cast(int, x), CTRL, 0xf, 0xf, true));
}
constexpr int XOR1 = 0xB1, XOR2 = 0x4E, HMIR = 0x141, ROR4 = 0x124, ROR8 = 0x128, NBC4 = 0x154;
__device__ __forceinline__ float fmx(float a, float b) { float r; asm("v_max_f32 %0, %1, %2" : "=v"(r) : "v"(a), "v"(b)); return r; }
__device__ __forceinline__ float xrow_max(float x) {
    auto s = __builtin_amdgcn_permlane16_swap(__float_as_uint(x), __float_as_uint(x), false, false);
    x = fmx(__uint_as_float(s[0]), __uint_as_float(s[1]));
    auto t = __builtin_amdgcn_permlane32_swap(__float_as_uint(x), __float_as_uint(x), false, false);
    return fmx(__uint_as_float(t[0]), __uint_as_float(t[1]));
}
__device__ __forceinline__ float x32_sum(float x) {
    auto t = __builtin_amdgcn_permlane32_swap(__float_as_uint(x), __float_as_uint(x), false, false);
    return __uint_as_float(t[0]) + __uint_as_float(t[1]);
}
__device__ __forceinline__ float x16_sum(float x) {
    auto s = __builtin_amdgcn_permlane16_swap(__float_as_uint(x), __float_as_uint(x), false, false);
    return __uint_as_float(s[0]) + __uint_as_float(s[1]);
}
__device__ __forceinline__ float xrow_sum(float x) {
    auto s = __builtin_amdgcn_permlane16_swap(__float_as_uint(x), __float_as_uint(x), false, false);
    return x32_sum(__uint_as_float(s[0]) + __uint_as_float(s[1]));
}
__device__ __forceinline__ float par16_sum(float x) { x += dpp<XOR2>(x); x += dpp<ROR4>(x); return x + dpp<ROR8>(x); }
__device__ __forceinline__ float bperm(int srclane, float v) { return __builtin_bit_cast(float, __builtin_amdgcn_ds_bpermute(srclane * 4, __builtin_bit_cast(int, v))); }

__device__ __forceinline__ void wait_lds_ge(unsigned addr, unsigned want) {
#pragma unroll 1
    for (unsigned i = 0; i < (1u << 22); ++i) { if ((unsigned)__builtin_amdgcn_readfirstlane(*(volatile LAS unsigned*)addr) >= want) break; __builtin_amdgcn_s_sleep(1); }
    cbar();
}
__device__ __forceinline__ void spin_lds_ge(unsigned addr, unsigned want) {
#pragma unroll 1
    for (unsigned i = 0; i < (1u << 24); ++i) { if ((unsigned)__builtin_amdgcn_readfirstlane(*(volatile LAS unsigned*)addr) >= want) break; }
    cbar();
}
__device__ __forceinline__ void wait_prog(unsigned want) {
#pragma unroll 1
    for (unsigned i = 0; i < (1u << 22); ++i) {
        const v4u p = *(volatile LAS v4u*)L_F;
        const unsigned m01 = p.x < p.y ? p.x : p.y, m23 = p.z < p.w ? p.z : p.w;
        if ((unsigned)__builtin_amdgcn_readfirstlane(m01 < m23 ? m01 : m23) >= want) break;
        __builtin_amdgcn_s_sleep(1);
    }
    cbar();
}
__device__ __forceinline__ void wait_prog_c(unsigned want, unsigned& seen) {
#pragma unroll 1
    for (unsigned i = 0; i < (1u << 24) && seen < want; ++i) {
        const v4u p = *(volatile LAS v4u*)L_F;
        const unsigned m01 = p.x < p.y ? p.x : p.y, m23 = p.z < p.w ? p.z : p.w;
        seen = (unsigned)__builtin_amdgcn_readfirstlane(m01 < m23 ? m01 : m23);
    }
    cbar();
}
__device__ __forceinline__ void lds_signal(unsigned addr) {
    asm volatile("s_waitcnt lgkmcnt(0)\n\ts_mov_b64 exec, 1\n\tds_add_u32 %0, %1\n\ts_mov_b64 exec, -1" :: "v"(addr), "v"(1u) : "memory");
}
__device__ __forceinline__ bool tags_ok(v4u g, unsigned t0, unsigned t1) { return (g.y == t0) & (g.w == t1); }
__device__ __forceinline__ v4u ldg(__amdgpu_buffer_rsrc_t rs, int off) { return __builtin_amdgcn_raw_buffer_load_b128(rs, off, 0, AUX_SC1); }
__device__ __forceinline__ void stg(__amdgpu_buffer_rsrc_t rs, int off, unsigned p0, unsigned p1, unsigned t0, unsigned t1) { __builtin_amdgcn_raw_buffer_store_b128(v4u{p0, t0, p1, t1}, rs, off, 0, AUX_SC1); }

#define STAMP(i) do { } while (0)
#define NPOLL(i, v) do { } while (0)
#define STAMP_DECL do { } while (0)
#define STAMP_FLUSH() do { } while (0)

__global__ void __launch_bounds__(512, 2) fused(Args a) {
    const int tid = threadIdx.x, lane = tid & 63, w = __builtin_amdgcn_readfirstlane(tid >> 6);
    const int blk = blockIdx.x, kvh = blk & 7, j = blk >> 3, b = j >> 3, s = j & 7;
    const bool hrole = blk < 196, owner = j < 16;
    const int own = kvh * 16 + j;
    const bool mlpo = owner && own < 112;
    const int nq = b < 2 ? 4 : 3;
    const unsigned nW = 1 + nq + (hrole ? 2 : 0);
    STAMP_DECL;
    STAMP(0);
    {
        unsigned long long pc;
        asm volatile("s_getpc_b64 %0" : "=s"(pc));
        pc = (pc & ~0xFFull) + w * 1376;
        dma16<false>(lane * 16, pc, L_PF);
        dma16<false>(lane * 16, pc + 1024, L_PF);
    }
    if (__builtin_expect(w < 4, 1)) {
        const int L = w;
        if (lane < 56) dma16<false>(lane * 16, (unsigned long long)a.x + L * 896, L_X + L * 912);
        const unsigned f = ((lane >> 4) & 1) | ((L & 1) << 1), sp = lane & 7, gp = (((sp >> 1) ^ f) << 1) | (sp & 1);
        {
            const int r0 = b < 2 ? b * 128 : 256 + (b - 2) * 96;
            unsigned long long p = (unsigned long long)a.wq + (size_t)(r0 + 8 * L) * 8192 + (kvh * 512 + s * 64) * 2; unsigned l = L_WQ + L * 1024;
            const unsigned voff = (lane >> 3) * 8192 + gp * 16;
#pragma unroll 1
            for (int i = 0; i < nq; ++i) { dma16<true>(voff, p, l); p += 32 * 8192; l += 4096; }
        }
        if (hrole) {
            const int cb = blk % 28, rb = blk / 28;
            unsigned long long p = (unsigned long long)a.w1 + (size_t)(rb * 64 + 8 * L) * 3584 + cb * 128;
            const unsigned voff = (lane >> 3) * 3584 + gp * 16;
            dma16<true>(voff, p, L_W1 + L * 1024);
            dma16<true>(voff, p + 32 * 3584, L_W1 + L * 1024 + 4096);
        }
        const size_t kvbase = ((size_t)(b * 8 + kvh) * 4096 + 3072) * 512 + (size_t)s * (BS * 1024) + L * 1024;
#define KVSTEP(i) ((((i) + 1) * 4 % BS == 0) ? (7 * BS + 4) * 1024 : 4096)
        unsigned n = nW;
        volatile LAS unsigned* prog = (volatile LAS unsigned*)(L_F + 4 * L);
        {
            const unsigned voff = (lane >> 5) * 512 + ((lane & 31) ^ (lane >> 5)) * 16;
            unsigned long long p = (unsigned long long)a.k + kvbase; unsigned l = L_K + L * 1056;
            dma16<true>(voff, p, l); ++n; p += KVSTEP(0); l += 4224;
            __builtin_amdgcn_s_barrier();
#pragma unroll 1
            for (int i = 1; i < 16; ++i) { dma16<true>(voff, p, l); ++n; waitv<WINK - 1>(); const int d = (int)n - (WINK - 1); *prog = d > 0 ? d : 0; p += KVSTEP(i); l += 4224; }
        }
        STAMP(1);
        {
            const unsigned voff = (lane >> 5) * 512 + ((lane & 31) ^ ((lane >> 5) << 1)) * 16;
            unsigned long long p = (unsigned long long)a.v + kvbase; unsigned l = L_V + L * 1088;
#pragma unroll 1
            for (int i = 0; i < 16; ++i) {
                if (i == 12) wait_lds_ge(F_S1, 4);
                dma16<true>(voff, p, l); ++n;
                if (i < WINSW) { waitv<WINK - 1>(); *prog = n - (WINK - 1); } else { waitv<WINL - 1>(); *prog = n - (WINL - 1); }
                p += KVSTEP(i); l += 4352;
            }
        }
        STAMP(2);
        if (owner) {
            wait_lds_ge(F_P, 4);
            const unsigned voff = lane * 16;
            unsigned l = L_K + L * 928;
            const unsigned long long w2p = (unsigned long long)a.w2 + (unsigned)(((mlpo ? own : 0) * 16 + L) * 896);
            const unsigned long long wop = (unsigned long long)a.wo + (unsigned)((own * 32 + L) * 896);
            if (lane < 56) {
#pragma unroll
                for (int i = 0; i < 4; ++i) dma16<true>(voff, w2p + i * 4 * 896, l + i * 3712);
            }
            waitv<8>(); *prog = n - 4;
            waitv<4>(); *prog = n;
            STAMP(4);
            if (lane < 56) {
#pragma unroll
                for (int i = 0; i < 8; ++i) dma16<true>(voff, wop + i * 4 * 896, l + (4 + i) * 3712);
            }
            waitv<8>(); *prog = n + 4;
            waitv<0>(); *prog = n + 12;
        } else {
            waitv<4>(); *prog = n - 4;
            waitv<0>(); *prog = n;
            STAMP(4);
        }
        STAMP(3);
        STAMP_FLUSH();
        return;
    }
    const int t = w - 4, n = lane & 15, g = lane >> 4;
    __builtin_amdgcn_s_setprio(SETPRIO);
    if (t == 0) {
        if (lane < 20) *(LAS unsigned*)(L_Z + 4 * lane) = 0u;
        if (lane < 56) *(LAS v4u*)(L_X + 4 * 912 + lane * 16) = v4u{0x3F803F80u, 0x3F803F80u, 0x3F803F80u, 0x3F803F80u};
    }
    asm volatile("s_waitcnt lgkmcnt(0)" ::: "memory");
    __builtin_amdgcn_s_barrier();
    cbar();
    const __amdgpu_buffer_rsrc_t rs = __builtin_amdgcn_make_buffer_rsrc(a.ws, 0, WS_END, 0x00020000);
    unsigned Ev = __hip_atomic_load((GAS unsigned*)(a.ws + WS_E), __ATOMIC_RELAXED, __HIP_MEMORY_SCOPE_AGENT);
    v2u b1v = v2u{0u, 0u};
    if (mlpo && lane < 32) b1v = *(const GAS v2u*)(a.b1 + (own * 16 + (lane & 3) * 4) * 2);
    const int fi = blk - 128, fb = fi / 28, fc = fi % 28;
    const bool fin = fi >= 0 && fi < 112;
    v2u xres = v2u{0u, 0u};
    if (t == 3 && fin && lane < 4) xres = *(const GAS v2u*)(a.x + (fb * 448 + 16 * fc + 4 * lane) * 2);
    const unsigned r1 = n >> 2, c4 = n & 3;

    unsigned Eu, T0, T1;
    {
        const unsigned xa = L_X + (n < 4 ? n : 4) * 912 + g * 16;
        const unsigned fs = ((r1 >> 1) & 1) | ((g & 1) << 1);
        const unsigned wo_ = (8 * g + r1) * 128 + ((t ^ fs) * 32) + c4 * 8;
        const int ks0 = b < 2 ? b * 4 : 8 + (b - 2) * 3, rb = blk / 28;
        const int qoff = WS_Q + (((n * 8 + kvh) * 4 + b) * 128 + s * 16 + t * 4 + g) * 16;
        const int hoff = WS_H + ((n * 7 + rb) * 448 + (blk % 28) * 16 + t * 4 + g) * 16;
        wait_prog(1);
        v4u xf[14];
#pragma unroll
        for (int ks = 0; ks < 14; ++ks) xf[ks] = lds128(xa + 64 * ks);
        v4u bq[4], bh[2];
#pragma unroll
        for (int kk = 0; kk < 4; ++kk) bq[kk] = lds128(xa + 64 * (ks0 + (kk < nq ? kk : 0)));
#pragma unroll
        for (int kk = 0; kk < 2; ++kk) bh[kk] = lds128(xa + 64 * ((hrole ? 2 * rb : 0) + kk));
        __builtin_amdgcn_sched_barrier(0);
        f32x4 G0 = {0.f, 0.f, 0.f, 0.f}, G1 = {0.f, 0.f, 0.f, 0.f};
#pragma unroll
        for (int ks = 0; ks < 14; ks += 2) { G0 = mfma(xf[ks], xf[ks], G0); G1 = mfma(xf[ks + 1], xf[ks + 1], G1); }
        const f32x4 G = G0 + G1;
        const float gsel = n == 0 ? G[0] : n == 1 ? G[1] : n == 2 ? G[2] : G[3];
        const float sq = bperm(n & 3, gsel), sm = bperm(16 + (n & 3), G[0]);
        const float mean = sm * (1.0f / 448.0f);
        const float var = fmaxf(fmaf(sq, 1.0f / 448.0f, -mean * mean), 0.f);
        const float rstd = __builtin_amdgcn_rsqf(var + 1e-5f), mr = mean * rstd;
        asm volatile("" :: "v"(rstd), "v"(mr), "v"(qoff), "v"(hoff));
        wait_prog(nW);
        asm volatile("" : "+v"(Ev));
        Eu = __builtin_amdgcn_readfirstlane(Ev);
        T0 = (Eu * 0x9E3779B1u + 0x7F4A7C15u) & 0xBFFFBFFFu; T1 = ((Eu ^ 0x5BD1E995u) * 0x85EBCA6Bu + 0x165667B1u) & 0xBFFFBFFFu;
        STAMP(1);
        v4u aq[4], ah[2];
#pragma unroll
        for (int kk = 0; kk < 4; ++kk) aq[kk] = trfrag(L_WQ + wo_ + kk * 4096, L_WQ + wo_ + kk * 4096 + 512);
#pragma unroll
        for (int kk = 0; kk < 2; ++kk) ah[kk] = trfrag(L_W1 + wo_ + kk * 4096, L_W1 + wo_ + kk * 4096 + 512);
        __builtin_amdgcn_sched_barrier(0);
        f32x4 Dq = {0.f, 0.f, 0.f, 0.f}, Dq2 = {0.f, 0.f, 0.f, 0.f}, Dh = {0.f, 0.f, 0.f, 0.f};
        Dq = mfma(aq[0], bq[0], Dq);
        Dq2 = mfma(aq[1], bq[1], Dq2);
        Dh = mfma(ah[0], bh[0], Dh);
        Dq = mfma(aq[2], bq[2], Dq);
        if (nq == 4) Dq2 = mfma(aq[3], bq[3], Dq2);
        Dh = mfma(ah[1], bh[1], Dh);
        Dq += Dq2;
        lds_signal(F_S1);
        float q[4], h[4];
#pragma unroll
        for (int r = 0; r < 4; ++r) { q[r] = fmaf(rstd, Dq[r], -mr * dpp<NBC4>(Dq[r])); h[r] = fmaf(rstd, Dh[r], -mr * dpp<NBC4>(Dh[r])); }
        if (n < 4) {
            stg(rs, qoff, cvtpk(q[0], q[1]), cvtpk(q[2], q[3]), T0, T1);
            if (hrole) stg(rs, hoff, cvtpk(h[0], h[1]), cvtpk(h[2], h[3]), T0, T1);
        }
    }
    STAMP(2);
    const int gid = b * 8 + kvh;
    const unsigned ka = L_K + t * 16896 + (n >> 1) * 1056 + (n & 1) * 512 + ((g ^ (n & 1)) * 16);
    v4u kf[16];
    v4u hx = v4u{0u, 0u, 0u, 0u};
    const bool hval = mlpo && lane < 28;
    {
        const int qo = WS_Q + (gid * 512 + (lane >> 5) * 128 + 32 * t + (lane & 31)) * 16;
        const int ho = hval ? WS_H + ((t * 7 + (lane >> 2)) * 448 + own * 4 + (lane & 3)) * 16 : qo;
        v4u x0, x1;
        wait_prog(nW + QPOLL);
        if (4 * (t + 1) <= QPOLL) {
#pragma unroll
            for (int i = 0; i < 16; ++i) kf[i] = lds128(ka + (i >> 3) * 8448 + (i & 7) * 64);
        }
#pragma unroll 1
        for (unsigned it = 0; it < SPIN_CAP; ++it) {
            cbar();
            NPOLL(9, 1);
            x0 = ldg(rs, qo); x1 = ldg(rs, qo + 256 * 16); hx = ldg(rs, ho);
            if (__all(tags_ok(x0, T0, T1) & tags_ok(x1, T0, T1) & (!hval | tags_ok(hx, T0, T1)))) break;
        }
        STAMP(3);
        const float q0 = x32_sum(bflo(x0.x) + bflo(x1.x)), q1 = x32_sum(bfhi(x0.x) + bfhi(x1.x));
        const float q2 = x32_sum(bflo(x0.z) + bflo(x1.z)), q3 = x32_sum(bfhi(x0.z) + bfhi(x1.z));
        if (lane < 32) *(LAS v2u*)(L_Q + (32 * t + lane) * 8) = v2u{cvtpk(q0, q1), cvtpk(q2, q3)};
        lds_signal(F_Q);
    }
    {
        if (4 * (t + 1) > QPOLL) {
            wait_prog(nW + 4 * (t + 1));
#pragma unroll
            for (int i = 0; i < 16; ++i) kf[i] = lds128(ka + (i >> 3) * 8448 + (i & 7) * 64);
        }
        spin_lds_ge(F_Q, 4);
        v4u qf[8];
#pragma unroll
        for (int ks = 0; ks < 8; ++ks) qf[ks] = lds128(L_Q + (n & 1) * 512 + ks * 64 + g * 16);
        __builtin_amdgcn_sched_barrier(0);
        f32x4 z0 = {0.f, 0.f, 0.f, 0.f}, z1 = {0.f, 0.f, 0.f, 0.f};
#pragma unroll
        for (int ks = 0; ks < 8; ++ks) { z0 = mfma(kf[ks], qf[ks], z0); z1 = mfma(kf[8 + ks], qf[ks], z1); }
        constexpr float C = 0.0625f * 1.4426950408889634f;
        const float z[4] = {(n & 2) ? z1[0] : z0[0], (n & 2) ? z1[1] : z0[1], (n & 2) ? z1[2] : z0[2], (n & 2) ? z1[3] : z0[3]};
        float m = fmx(fmx(z[0], z[1]), fmx(z[2], z[3])) * C;
        m = xrow_max(fmx(m, dpp<XOR2>(m)));
        float p[4];
#pragma unroll
        for (int r = 0; r < 4; ++r) p[r] = __builtin_amdgcn_exp2f(fmaf(z[r], C, -m));
        if (n < 4) *(LAS v2u*)(L_P + (n & 1) * 256 + (32 * t + 8 * g + 4 * (n >> 1)) * 2) = v2u{cvtpk(p[0], p[1]), cvtpk(p[2], p[3])};
        float l = (p[0] + p[1]) + (p[2] + p[3]);
        l = xrow_sum(l + dpp<XOR2>(l));
        if (lane < 2) { *(LAS float*)(L_MX + lane * 16 + t * 4) = m; *(LAS float*)(L_LS + lane * 16 + t * 4) = l; }
        lds_signal(F_P);
    }
    STAMP(4);
    if (mlpo) {
        const float hv[4] = {hval ? bflo(hx.x) : 0.f, hval ? bfhi(hx.x) : 0.f, hval ? bflo(hx.z) : 0.f, hval ? bfhi(hx.z) : 0.f};
        const float bb[4] = {bflo(b1v.x), bfhi(b1v.x), bflo(b1v.y), bfhi(b1v.y)};
        float act[4];
#pragma unroll
        for (int r = 0; r < 4; ++r) {
            float v = hv[r]; v += dpp<ROR8>(v); v += dpp<ROR4>(v); v = x16_sum(v) + bb[r];
            const float e = __builtin_amdgcn_exp2f(-1.4426950408889634f * fabsf(v));
            const float num = v > 0.f ? fmaf(2.f, e, 1.f) : e * (e + 2.f);
            const float den = v > 0.f ? fmaf(2.f * e, e, num) : num + 2.f;
            act[r] = v * num * __builtin_amdgcn_rcpf(den);
        }
        if (lane < 4) *(LAS v2u*)(L_AM + t * 32 + lane * 8) = v2u{cvtpk(act[0], act[1]), cvtpk(act[2], act[3])};
        lds_signal(F_AM);
    }
    {
        const unsigned e = r1 & 1;
        const unsigned vb = L_V + (2 * g + (r1 >> 1)) * 1088 + e * 512 + t * 128 + c4 * 8;
        const unsigned va0 = vb + e * 32, va1 = vb + (e ^ 1) * 32;
        const int ooff = WS_O + ((((kvh * 16 + (n & 1) * 8 + 2 * t + ((n >> 2) & 1)) * 4 + b) * 8 + s) * 8 + 4 * ((n >> 1) & 1) + g) * 16;
        const int soff = WS_S + ((((kvh * 2 + (lane & 1)) * 4 + b) * 8 + s) * 4 + ((lane >> 1) & 3)) * 16;
        int ooffp = ooff, soffp = soff;
        asm volatile("" : "+v"(ooffp), "+v"(soffp));
        spin_lds_ge(F_P, 4);
        v4u pf[4];
#pragma unroll
        for (int kk = 0; kk < 4; ++kk) pf[kk] = lds128(L_P + (n & 1) * 256 + kk * 64 + g * 16);
        const v4u mx = lds128(L_MX + (n & 1) * 16), ls = lds128(L_LS + (n & 1) * 16);
        const float mk[4] = {__uint_as_float(mx.x), __uint_as_float(mx.y), __uint_as_float(mx.z), __uint_as_float(mx.w)};
        const float mW = fmx(fmx(mk[0], mk[1]), fmx(mk[2], mk[3]));
        float fk[4];
#pragma unroll
        for (int kk = 0; kk < 4; ++kk) fk[kk] = __builtin_amdgcn_exp2f(mk[kk] - mW);
        const float lW = fmaf(fk[0], __uint_as_float(ls.x), fk[1] * __uint_as_float(ls.y)) + fmaf(fk[2], __uint_as_float(ls.z), fk[3] * __uint_as_float(ls.w));
        if (t == 0 && lane < 8) stg(rs, soffp, __float_as_uint(mW), __float_as_uint(lW), T0, T1);
        f32x4 o[4];
        unsigned seen = 0;
        v4u vf[2][4];
        wait_prog_c(nW + 20, seen);
#pragma unroll
        for (int aa = 0; aa < 4; ++aa) { const unsigned ad = ((aa & 1) ? va1 : va0) + (aa >> 1) * 64; vf[0][aa] = trfrag(ad, ad + 8704); }
#pragma unroll
        for (int kk = 0; kk < 4; ++kk) {
            if (kk < 3) {
                wait_prog_c(nW + 16 + 4 * (kk + 2), seen);
#pragma unroll
                for (int aa = 0; aa < 4; ++aa) { const unsigned ad = ((aa & 1) ? va1 : va0) + (aa >> 1) * 64 + (kk + 1) * 17408; vf[(kk + 1) & 1][aa] = trfrag(ad, ad + 8704); }
            }
#pragma unroll
            for (int aa = 0; aa < 4; ++aa) { const f32x4 d = mfma(vf[kk & 1][aa], pf[kk], f32x4{0.f, 0.f, 0.f, 0.f}); o[aa] = kk == 0 ? fk[0] * d : o[aa] + fk[kk] * d; }
            asm volatile("" : "+v"(o[0]), "+v"(o[1]), "+v"(o[2]), "+v"(o[3]));
        }
        float v[4];
#pragma unroll
        for (int r = 0; r < 4; ++r) { const float lo = (n & 2) ? o[1][r] : o[0][r], hi = (n & 2) ? o[3][r] : o[2][r]; v[r] = (n & 4) ? hi : lo; }
        if (n < 8) stg(rs, ooffp, cvtpk(v[0], v[1]), cvtpk(v[2], v[3]), T0, T1);
    }
    STAMP(5);
    if (owner) {
        const unsigned ba = L_K + (4 * g + r1) * 928 + t * 224 + c4 * 8;
        const int q4 = n >> 2, h = j >> 3;
        const int oo = WS_O + ((own * 4 + t) * 64 + lane) * 16, so = WS_S + (((kvh * 2 + h) * 4 + t) * 8 + (lane >> 3)) * 64;
        const int foff0 = WS_FIN + ((((n & 3) * 28 + 7 * t + q4) * 128 + own) * 4 + g) * 16;
        unsigned ab[4];
#pragma unroll
        for (int q = 0; q < 4; ++q) ab[q] = q4 == q ? L_A + (n & 3) * 64 + g * 16 : L_Z;
        const v4u sel = v4u{n == 0 ? 0x00003F80u : n == 1 ? 0x3F800000u : 0u, 0u, n == 2 ? 0x00003F80u : n == 3 ? 0x3F800000u : 0u, 0u};
        v4u selp = sel;
        int foffp = foff0;
        unsigned aw = L_A + t * 64 + (8 * (lane & 3) + 4 * (lane >> 2)) * 2;
        asm volatile("" : "+v"(selp), "+v"(foffp), "+v"(aw), "+v"(ab[0]), "+v"(ab[1]), "+v"(ab[2]), "+v"(ab[3]));
        f32x4 acc0 = {0.f, 0.f, 0.f, 0.f}, acc1 = {0.f, 0.f, 0.f, 0.f};
        v4u wf[7], og, sg;
        float wgt = 0.f;
        wait_prog(nW + 32 + 4);
        if (mlpo) {
            wait_lds_ge(F_AM, 4);
            const v2u am = lds64(L_AM + (n & 3) * 32 + g * 8);
#pragma unroll
            for (int T = 0; T < 7; ++T) {
                const v2u wl = tr64(ba + T * 32);
                const bool on = q4 == (T & 3);
                const f32x4 d = mfma(v4u{wl.x, wl.y, 0u, 0u}, v4u{on ? am.x : 0u, on ? am.y : 0u, 0u, 0u}, T < 4 ? acc0 : acc1);
                if (T < 4) acc0 = d; else acc1 = d;
            }
        }
        wait_prog(nW + 32 + 12);
#pragma unroll
        for (int T = 0; T < 7; ++T) wf[T] = trfrag(ba + 16 * 928 + T * 32, ba + 32 * 928 + T * 32);
        bool havew = false;
        f32x4 ds;
#pragma unroll 1
        for (unsigned it = 0; it < SPIN_CAP; ++it) {
            cbar();
            og = ldg(rs, oo);
            if (!havew) {
                sg = ldg(rs, so);
                if (__all(tags_ok(sg, T0, T1))) {
                    const float m = __uint_as_float(sg.x), l = __uint_as_float(sg.z);
                    const float M = xrow_max(fmx(m, dpp<ROR8>(m))), e = __builtin_amdgcn_exp2f(m - M);
                    float le = l * e; le += dpp<ROR8>(le);
                    wgt = e * __builtin_amdgcn_rcpf(xrow_sum(le));
                    havew = true;
                }
            }
            const v4u pb = v4u{cvtpk(bflo(og.x) * wgt, bfhi(og.x) * wgt), 0u, cvtpk(bflo(og.z) * wgt, bfhi(og.z) * wgt), 0u};
            ds = mfma(selp, pb, f32x4{0.f, 0.f, 0.f, 0.f});
            if (havew && __all(tags_ok(og, T0, T1))) break;
            if (OSLEEP) __builtin_amdgcn_s_sleep(OSLEEP);
        }
        STAMP(6);
        float v[4];
#pragma unroll
        for (int r = 0; r < 4; ++r) v[r] = ds[r] + dpp<ROR8>(ds[r]);
        if (lane < 8) *(LAS v2u*)aw = v2u{cvtpk(v[0], v[1]), cvtpk(v[2], v[3])};
        lds_signal(F_A);
        spin_lds_ge(F_A, 4);
        v4u B[4];
#pragma unroll
        for (int q = 0; q < 4; ++q) B[q] = lds128(ab[q]);
        __builtin_amdgcn_sched_barrier(0);
#pragma unroll
        for (int T = 0; T < 4; ++T) { acc0 = mfma(wf[T], B[T], acc0); if (T < 3) acc1 = mfma(wf[4 + T], B[T], acc1); }
        stg(rs, foffp, cvtpk(acc0[0], acc0[1]), cvtpk(acc0[2], acc0[3]), T0, T1);
        if (q4 < 3) stg(rs, foffp + 4 * 128 * 4 * 16, cvtpk(acc1[0], acc1[1]), cvtpk(acc1[2], acc1[3]), T0, T1);
    }
    STAMP(7);
    if (t == 3 && fin) {
        const int fo = WS_FIN + ((fb * 28 + fc) * 512 + lane) * 16;
        v4u sel = v4u{n == 0 ? 0x00003F80u : n == 1 ? 0x3F800000u : 0u, 0u, n == 2 ? 0x00003F80u : n == 3 ? 0x3F800000u : 0u, 0u};
        float xr[4] = {bflo(xres.x), bfhi(xres.x), bflo(xres.y), bfhi(xres.y)};
        unsigned long long op = (unsigned long long)(a.out + fb * 448 + 16 * fc + 4 * (lane & 3));
        asm volatile("" : "+v"(sel), "+v"(xr[0]), "+v"(xr[1]), "+v"(xr[2]), "+v"(xr[3]), "+v"(op));
        {
#pragma unroll 1
            for (unsigned it = 0; it < SPIN_CAP; ++it) {
                cbar();
                bool ok = true;
                if (lane < 8) ok = tags_ok(ldg(rs, WS_FIN + ((fb * 28 + fc) * 512 + (lane * 16 + PROBEJ) * 4) * 16), T0, T1);
                if (__all(ok)) break;
                __builtin_amdgcn_s_sleep(FPROBE);
            }
            if (PSLEEP) __builtin_amdgcn_s_sleep(PSLEEP);
        }
        f32x4 d0, d1;
#pragma unroll 1
        for (unsigned it = 0; it < SPIN_CAP; ++it) {
            cbar();
            v4u f[8];
            bool ok = true;
#pragma unroll
            for (int i = 0; i < 8; ++i) f[i] = ldg(rs, fo + i * 1024);
            d0 = f32x4{0.f, 0.f, 0.f, 0.f}; d1 = f32x4{0.f, 0.f, 0.f, 0.f};
#pragma unroll
            for (int i = 0; i < 8; i += 2) {
                ok &= tags_ok(f[i], T0, T1) & tags_ok(f[i + 1], T0, T1);
                d0 = mfma(sel, f[i], d0); d1 = mfma(sel, f[i + 1], d1);
            }
            if (__all(ok)) break;
        }
        float v[4];
#pragma unroll
        for (int r = 0; r < 4; ++r) { float x = d0[r] + d1[r]; x += dpp<ROR8>(x); v[r] = x + dpp<ROR4>(x); }
        if (lane < 4) {
            *(GAS v2u*)op = v2u{cvtpk(xr[0] + v[0], xr[1] + v[1]), cvtpk(xr[2] + v[2], xr[3] + v[3])};
        }
        if (blk == 128 && lane == 0) __hip_atomic_store((GAS unsigned*)(a.ws + WS_E), Eu + 1u, __ATOMIC_RELAXED, __HIP_MEMORY_SCOPE_AGENT);
        STAMP(8);
    }
    STAMP_FLUSH();
}
}

extern "C" void kernel_launch(void* const* d_in, const int* in_sizes, int n_in, void* d_out, int out_size, void* d_ws, size_t ws_size, hipStream_t stream) {
    (void)in_sizes; (void)n_in; (void)out_size; (void)ws_size;
    fk::Args a;
    a.x = (const unsigned char*)d_in[0]; a.k = (const unsigned char*)d_in[1]; a.v = (const unsigned char*)d_in[2];
    a.wq = (const unsigned char*)d_in[3]; a.wo = (const unsigned char*)d_in[4]; a.w1 = (const unsigned char*)d_in[6];
    a.b1 = (const unsigned char*)d_in[7]; a.w2 = (const unsigned char*)d_in[8]; a.out = (unsigned short*)d_out; a.ws = (unsigned char*)d_ws;
    (void)hipFuncSetAttribute((const void*)fk::fused, hipFuncAttributeMaxDynamicSharedMemorySize, fk::LDS_BYTES);
    hipLaunchKernelGGL(fk::fused, dim3(256), dim3(512), fk::LDS_BYTES, stream, a);
}
```

```cpp
#include <hip/hip_runtime.h>
#include <stdint.h>

#define GAS __attribute__((address_space(1)))
#define LAS __attribute__((address_space(3)))
typedef unsigned v4u __attribute__((ext_vector_type(4)));
typedef unsigned v2u __attribute__((ext_vector_type(2)));
typedef float f32x4 __attribute__((ext_vector_type(4)));
typedef float f32x2 __attribute__((ext_vector_type(2)));
typedef short s16x4 __attribute__((ext_vector_type(4)));
typedef __bf16 bf16x8_t __attribute__((ext_vector_type(8)));
typedef __bf16 bf16x2_t __attribute__((ext_vector_type(2)));

namespace fk {
constexpr int WINK = 6, WINL = 8;
constexpr int WINSW = 5;
constexpr int BS = 4;
constexpr int QPOLL = 14;
constexpr int FPROBE = 6;
constexpr int AUX_SC1 = 16;
constexpr int L_K = 0;
constexpr int L_WQ = 67584;
constexpr int L_V = L_WQ + 16384;
constexpr int L_X = L_V + 69632 - 8192 - 4608;
constexpr int L_W1 = L_V + 69632 - 8192;
constexpr int L_M = L_V + 69632;
constexpr int L_Q = L_M;
constexpr int L_P = L_Q + 1024;
constexpr int L_A = L_P + 512;
constexpr int L_AM = L_A + 256;
constexpr int L_MX = L_AM + 128;
constexpr int L_LS = L_MX + 32;
constexpr int L_Z = L_LS + 32;
constexpr int L_F = L_Z + 16;
constexpr int F_S1 = L_F + 16, F_P = L_F + 20, F_Q = L_F + 24, F_A = L_F + 28, F_AM = L_F + 32;
constexpr int L_PF = L_F + 64;
constexpr int LDS_BYTES = L_PF + 1024;
static_assert(L_X >= L_V + 52 * 1088, "X / W1 images alias V instructions >= 52 only");
static_assert(48 * 928 <= 67584, "projection rows inside the K image");
static_assert(LDS_BYTES <= 163840, "LDS");
static_assert((L_F & 15) == 0 && (L_MX & 15) == 0 && (L_A & 15) == 0 && (L_Z & 15) == 0, "alignment");
constexpr int WS_E = 0;
constexpr int WS_Q = 4096;
constexpr int WS_H = WS_Q + 4 * 8 * 4 * 128 * 16;
constexpr int WS_O = WS_H + 4 * 7 * 448 * 16;
constexpr int WS_S = WS_O + 128 * 256 * 16;
constexpr int WS_FIN = WS_S + 512 * 64;
constexpr int WS_END = WS_FIN + 4 * 28 * 512 * 16;
constexpr unsigned SPIN_CAP = 1u << 16;

struct Args {
    const unsigned char* x; const unsigned char* k; const unsigned char* v; const unsigned char* wq; const unsigned char* wo;
    const unsigned char* w1; const unsigned char* b1; const unsigned char* w2; unsigned short* out; unsigned char* ws;
};

template <bool NT> __device__ __forceinline__ void dma16(unsigned voff, unsigned long long sbase, unsigned lds) {
    if (NT) asm volatile("s_mov_b32 m0, %2\n\ts_nop 0\n\tglobal_load_lds_dwordx4 %0, %1 nt" :: "v"(voff), "s"(sbase), "s"(lds) : "memory");
    else    asm volatile("s_mov_b32 m0, %2\n\ts_nop 0\n\tglobal_load_lds_dwordx4 %0, %1" :: "v"(voff), "s"(sbase), "s"(lds) : "memory");
}
template <int N> __device__ __forceinline__ void waitv() { asm volatile("s_waitcnt vmcnt(%0)" :: "i"(N) : "memory"); }
__device__ __forceinline__ void cbar() { asm volatile("" ::: "memory"); }

__device__ __forceinline__ f32x4 mfma(v4u a, v4u b, f32x4 c) {
    return __builtin_amdgcn_mfma_f32_16x16x32_bf16(__builtin_bit_cast(bf16x8_t, a), __builtin_bit_cast(bf16x8_t, b), c, 0, 0, 0);
}
__device__ __forceinline__ v4u lds128(unsigned a) { return *(const LAS v4u*)a; }
__device__ __forceinline__ v2u lds64(unsigned a) { return *(const LAS v2u*)a; }
__device__ __forceinline__ v2u tr64(unsigned a) { return __builtin_bit_cast(v2u, __builtin_amdgcn_ds_read_tr16_b64_v4i16((LAS s16x4*)a)); }
__device__ __forceinline__ v4u trfrag(unsigned a0, unsigned a1) { v2u lo = tr64(a0), hi = tr64(a1); return v4u{lo.x, lo.y, hi.x, hi.y}; }
__device__ __forceinline__ unsigned cvtpk(float lo, float hi) { return __builtin_bit_cast(unsigned, __builtin_convertvector(f32x2{lo, hi}, bf16x2_t)); }
__device__ __forceinline__ float bflo(unsigned u) { return __builtin_bit_cast(float, u << 16); }
__device__ __forceinline__ float bfhi(unsigned u) { return __builtin_bit_cast(float, u & 0xffff0000u); }
template <int CTRL> __device__ __forceinline__ float dpp(float x) {
    return __builtin_bit_cast(float, __builtin_amdgcn_mov_dpp(__builtin_bit_cast(int, x), CTRL, 0xf, 0xf, true));
}
constexpr int XOR2 = 0x4E, ROR4 = 0x124, ROR8 = 0x128, NBC4 = 0x154;
__device__ __forceinline__ float fmx(float a, float b) { float r; asm("v_max_f32 %0, %1, %2" : "=v"(r) : "v"(a), "v"(b)); return r; }
__device__ __forceinline__ float xrow_max(float x) {
    auto s = __builtin_amdgcn_permlane16_swap(__float_as_uint(x), __float_as_uint(x), false, false);
    x = fmx(__uint_as_float(s[0]), __uint_as_float(s[1]));
    auto t = __builtin_amdgcn_permlane32_swap(__float_as_uint(x), __float_as_uint(x), false, false);
    return fmx(__uint_as_float(t[0]), __uint_as_float(t[1]));
}
__device__ __forceinline__ float x32_sum(float x) {
    auto t = __builtin_amdgcn_permlane32_swap(__float_as_uint(x), __float_as_uint(x), false, false);
    return __uint_as_float(t[0]) + __uint_as_float(t[1]);
}
__device__ __forceinline__ float x16_sum(float x) {
    auto s = __builtin_amdgcn_permlane16_swap(__float_as_uint(x), __float_as_uint(x), false, false);
    return __uint_as_float(s[0]) + __uint_as_float(s[1]);
}
__device__ __forceinline__ float xrow_sum(float x) {
    auto s = __builtin_amdgcn_permlane16_swap(__float_as_uint(x), __float_as_uint(x), false, false);
    return x32_sum(__uint_as_float(s[0]) + __uint_as_float(s[1]));
}
__device__ __forceinline__ float bperm(int srclane, float v) { return __builtin_bit_cast(float, __builtin_amdgcn_ds_bpermute(srclane * 4, __builtin_bit_cast(int, v))); }

__device__ __forceinline__ void wait_lds_ge(unsigned addr, unsigned want) {
#pragma unroll 1
    for (unsigned i = 0; i < (1u << 19); ++i) { if ((unsigned)__builtin_amdgcn_readfirstlane(*(volatile LAS unsigned*)addr) >= want) break; __builtin_amdgcn_s_sleep(1); }
    cbar();
}
__device__ __forceinline__ void spin_lds_ge(unsigned addr, unsigned want) {
#pragma unroll 1
    for (unsigned i = 0; i < (1u << 20); ++i) { if ((unsigned)__builtin_amdgcn_readfirstlane(*(volatile LAS unsigned*)addr) >= want) break; }
    cbar();
}
__device__ __forceinline__ void wait_prog(unsigned want) {
#pragma unroll 1
    for (unsigned i = 0; i < (1u << 19); ++i) {
        const v4u p = *(volatile LAS v4u*)L_F;
        const unsigned m01 = p.x < p.y ? p.x : p.y, m23 = p.z < p.w ? p.z : p.w;
        if ((unsigned)__builtin_amdgcn_readfirstlane(m01 < m23 ? m01 : m23) >= want) break;
        __builtin_amdgcn_s_sleep(1);
    }
    cbar();
}
__device__ __forceinline__ void wait_prog_c(unsigned want, unsigned& seen) {
#pragma unroll 1
    for (unsigned i = 0; i < (1u << 20) && seen < want; ++i) {
        const v4u p = *(volatile LAS v4u*)L_F;
        const unsigned m01 = p.x < p.y ? p.x : p.y, m23 = p.z < p.w ? p.z : p.w;
        seen = (unsigned)__builtin_amdgcn_readfirstlane(m01 < m23 ? m01 : m23);
    }
    cbar();
}
__device__ __forceinline__ void lds_signal(unsigned addr) {
    asm volatile("s_waitcnt lgkmcnt(0)\n\ts_mov_b64 exec, 1\n\tds_add_u32 %0, %1\n\ts_mov_b64 exec, -1" :: "v"(addr), "v"(1u) : "memory");
}
__device__ __forceinline__ bool tags_ok(v4u g, unsigned t0, unsigned t1) { return (g.y == t0) & (g.w == t1); }
__device__ __forceinline__ v4u ldg(__amdgpu_buffer_rsrc_t rs, int off) { return __builtin_amdgcn_raw_buffer_load_b128(rs, off, 0, AUX_SC1); }
__device__ __forceinline__ void stg(__amdgpu_buffer_rsrc_t rs, int off, unsigned p0, unsigned p1, unsigned t0, unsigned t1) { __builtin_amdgcn_raw_buffer_store_b128(v4u{p0, t0, p1, t1}, rs, off, 0, AUX_SC1); }

__global__ void __launch_bounds__(512, 2) fused(Args a) {
    const int tid = threadIdx.x, lane = tid & 63, w = __builtin_amdgcn_readfirstlane(tid >> 6);
    const int blk = blockIdx.x, kvh = blk & 7, j = blk >> 3, b = j >> 3, s = j & 7;
    const bool hrole = blk < 196, owner = j < 16;
    const int own = kvh * 16 + j;
    const bool mlpo = owner && own < 112;
    const int nq = b < 2 ? 4 : 3;
    const unsigned nW = 1 + nq + (hrole ? 2 : 0);
    {
        unsigned long long pc;
        asm volatile("s_getpc_b64 %0" : "=s"(pc));
        pc = (pc & ~0xFFull) + w * 1376;
        dma16<false>(lane * 16, pc, L_PF);
        dma16<false>(lane * 16, pc + 1024, L_PF);
    }
    if (__builtin_expect(w < 4, 1)) {
        const int L = w;
        if (lane < 56) dma16<false>(lane * 16, (unsigned long long)a.x + L * 896, L_X + L * 912);
        const unsigned f = ((lane >> 4) & 1) | ((L & 1) << 1), sp = lane & 7, gp = (((sp >> 1) ^ f) << 1) | (sp & 1);
        {
            const int r0 = b < 2 ? b * 128 : 256 + (b - 2) * 96;
            unsigned long long p = (unsigned long long)a.wq + (size_t)(r0 + 8 * L) * 8192 + (kvh * 512 + s * 64) * 2; unsigned l = L_WQ + L * 1024;
            const unsigned voff = (lane >> 3) * 8192 + gp * 16;
#pragma unroll 1
            for (int i = 0; i < nq; ++i) { dma16<true>(voff, p, l); p += 32 * 8192; l += 4096; }
        }
        if (hrole) {
            const int cb = blk % 28, rb = blk / 28;
            unsigned long long p = (unsigned long long)a.w1 + (size_t)(rb * 64 + 8 * L) * 3584 + cb * 128;
            const unsigned voff = (lane >> 3) * 3584 + gp * 16;
            dma16<true>(voff, p, L_W1 + L * 1024);
            dma16<true>(voff, p + 32 * 3584, L_W1 + L * 1024 + 4096);
        }
        static_assert(BS == 4, "one stripe step per instruction of a loader");
        constexpr unsigned KVSTEP = 8 * BS * 1024;
        const size_t kvbase = ((size_t)(b * 8 + kvh) * 4096 + 3072) * 512 + (size_t)s * (BS * 1024) + L * 1024;
        unsigned n = nW;
        volatile LAS unsigned* prog = (volatile LAS unsigned*)(L_F + 4 * L);
        {
            const unsigned voff = (lane >> 5) * 512 + ((lane & 31) ^ (lane >> 5)) * 16;
            unsigned long long p = (unsigned long long)a.k + kvbase; unsigned l = L_K + L * 1056;
            dma16<true>(voff, p, l); ++n; p += KVSTEP; l += 4224;
            __builtin_amdgcn_s_barrier();
#pragma unroll 1
            for (int i = 1; i < 16; ++i) { dma16<true>(voff, p, l); ++n; waitv<WINK - 1>(); const int d = (int)n - (WINK - 1); *prog = d > 0 ? d : 0; p += KVSTEP; l += 4224; }
        }
        {
            const unsigned voff = (lane >> 5) * 512 + ((lane & 31) ^ ((lane >> 5) << 1)) * 16;
            unsigned long long p = (unsigned long long)a.v + kvbase; unsigned l = L_V + L * 1088;
#pragma unroll 1
            for (int i = 0; i < 16; ++i) {
                if (i == 12) wait_lds_ge(F_S1, 4);
                dma16<true>(voff, p, l); ++n;
                if (i < WINSW) { waitv<WINK - 1>(); *prog = n - (WINK - 1); } else { waitv<WINL - 1>(); *prog = n - (WINL - 1); }
                p += KVSTEP; l += 4352;
            }
        }
        if (owner) {
            wait_lds_ge(F_P, 4);
            const unsigned voff = lane * 16;
            unsigned l = L_K + L * 928;
            const unsigned long long w2p = (unsigned long long)a.w2 + (unsigned)(((mlpo ? own : 0) * 16 + L) * 896);
            const unsigned long long wop = (unsigned long long)a.wo + (unsigned)((own * 32 + L) * 896);
            if (lane < 56) {
#pragma unroll
                for (int i = 0; i < 4; ++i) dma16<true>(voff, w2p + i * 4 * 896, l + i * 3712);
            }
            waitv<8>(); *prog = n - 4;
            waitv<4>(); *prog = n;
            if (lane < 56) {
#pragma unroll
                for (int i = 0; i < 8; ++i) dma16<true>(voff, wop + i * 4 * 896, l + (4 + i) * 3712);
            }
            waitv<8>(); *prog = n + 4;
            waitv<0>(); *prog = n + 12;
        } else {
            waitv<4>(); *prog = n - 4;
            waitv<0>(); *prog = n;
        }
        return;
    }
    const int t = w - 4, n = lane & 15, g = lane >> 4;
    __builtin_amdgcn_s_setprio(3);
    if (t == 0) {
        if (lane < 20) *(LAS unsigned*)(L_Z + 4 * lane) = 0u;
        if (lane < 56) *(LAS v4u*)(L_X + 4 * 912 + lane * 16) = v4u{0x3F803F80u, 0x3F803F80u, 0x3F803F80u, 0x3F803F80u};
    }
    asm volatile("s_waitcnt lgkmcnt(0)" ::: "memory");
    __builtin_amdgcn_s_barrier();
    cbar();
    const __amdgpu_buffer_rsrc_t rs = __builtin_amdgcn_make_buffer_rsrc(a.ws, 0, WS_END, 0x00020000);
    unsigned Ev = __hip_atomic_load((GAS unsigned*)(a.ws + WS_E), __ATOMIC_RELAXED, __HIP_MEMORY_SCOPE_AGENT);
    v2u b1v = v2u{0u, 0u};
    if (mlpo && lane < 32) b1v = *(const GAS v2u*)(a.b1 + (own * 16 + (lane & 3) * 4) * 2);
    const int fi = blk - 128, fb = fi / 28, fc = fi % 28;
    const bool fin = fi >= 0 && fi < 112;
    v2u xres = v2u{0u, 0u};
    if (t == 3 && fin && lane < 4) xres = *(const GAS v2u*)(a.x + (fb * 448 + 16 * fc + 4 * lane) * 2);
    const unsigned r1 = n >> 2, c4 = n & 3;

    unsigned Eu, T0, T1;
    {
        const unsigned xa = L_X + (n < 4 ? n : 4) * 912 + g * 16;
        const unsigned fs = ((r1 >> 1) & 1) | ((g & 1) << 1);
        const unsigned wo_ = (8 * g + r1) * 128 + ((t ^ fs) * 32) + c4 * 8;
        const int ks0 = b < 2 ? b * 4 : 8 + (b - 2) * 3, rb = blk / 28;
        const int qoff = WS_Q + (((n * 8 + kvh) * 4 + b) * 128 + s * 16 + t * 4 + g) * 16;
        const int hoff = WS_H + ((n * 7 + rb) * 448 + (blk % 28) * 16 + t * 4 + g) * 16;
        wait_prog(1);
        v4u xf[14];
#pragma unroll
        for (int ks = 0; ks < 14; ++ks) xf[ks] = lds128(xa + 64 * ks);
        v4u bq[4], bh[2];
#pragma unroll
        for (int kk = 0; kk < 4; ++kk) bq[kk] = lds128(xa + 64 * (ks0 + (kk < nq ? kk : 0)));
#pragma unroll
        for (int kk = 0; kk < 2; ++kk) bh[kk] = lds128(xa + 64 * ((hrole ? 2 * rb : 0) + kk));
        __builtin_amdgcn_sched_barrier(0);
        f32x4 G0 = {0.f, 0.f, 0.f, 0.f}, G1 = {0.f, 0.f, 0.f, 0.f};
#pragma unroll
        for (int ks = 0; ks < 14; ks += 2) { G0 = mfma(xf[ks], xf[ks], G0); G1 = mfma(xf[ks + 1], xf[ks + 1], G1); }
        const f32x4 G = G0 + G1;
        const float gsel = n == 0 ? G[0] : n == 1 ? G[1] : n == 2 ? G[2] : G[3];
        const float sq = bperm(n & 3, gsel), sm = bperm(16 + (n & 3), G[0]);
        const float mean = sm * (1.0f / 448.0f);
        const float var = fmaxf(fmaf(sq, 1.0f / 448.0f, -mean * mean), 0.f);
        const float rstd = __builtin_amdgcn_rsqf(var + 1e-5f), mr = mean * rstd;
        asm volatile("" :: "v"(rstd), "v"(mr), "v"(qoff), "v"(hoff));
        wait_prog(nW);
        asm volatile("" : "+v"(Ev));
        Eu = __builtin_amdgcn_readfirstlane(Ev);
        T0 = (Eu * 0x9E3779B1u + 0x7F4A7C15u) & 0xBFFFBFFFu; T1 = ((Eu ^ 0x5BD1E995u) * 0x85EBCA6Bu + 0x165667B1u) & 0xBFFFBFFFu;
        v4u aq[4], ah[2];
#pragma unroll
        for (int kk = 0; kk < 4; ++kk) aq[kk] = trfrag(L_WQ + wo_ + kk * 4096, L_WQ + wo_ + kk * 4096 + 512);
#pragma unroll
        for (int kk = 0; kk < 2; ++kk) ah[kk] = trfrag(L_W1 + wo_ + kk * 4096, L_W1 + wo_ + kk * 4096 + 512);
        __builtin_amdgcn_sched_barrier(0);
        f32x4 Dq = {0.f, 0.f, 0.f, 0.f}, Dq2 = {0.f, 0.f, 0.f, 0.f}, Dh = {0.f, 0.f, 0.f, 0.f};
        Dq = mfma(aq[0], bq[0], Dq);
        Dq2 = mfma(aq[1], bq[1], Dq2);
        Dh = mfma(ah[0], bh[0], Dh);
        Dq = mfma(aq[2], bq[2], Dq);
        if (nq == 4) Dq2 = mfma(aq[3], bq[3], Dq2);
        Dh = mfma(ah[1], bh[1], Dh);
        Dq += Dq2;
        lds_signal(F_S1);
        float q[4], h[4];
#pragma unroll
        for (int r = 0; r < 4; ++r) { q[r] = fmaf(rstd, Dq[r], -mr * dpp<NBC4>(Dq[r])); h[r] = fmaf(rstd, Dh[r], -mr * dpp<NBC4>(Dh[r])); }
        if (n < 4) {
            stg(rs, qoff, cvtpk(q[0], q[1]), cvtpk(q[2], q[3]), T0, T1);
            if (hrole) stg(rs, hoff, cvtpk(h[0], h[1]), cvtpk(h[2], h[3]), T0, T1);
        }
    }
    const int gid = b * 8 + kvh;
    const unsigned ka = L_K + t * 16896 + (n >> 1) * 1056 + (n & 1) * 512 + ((g ^ (n & 1)) * 16);
    v4u kf[16];
    v4u hx = v4u{0u, 0u, 0u, 0u};
    const bool hval = mlpo && lane < 28;
    {
        const int qo = WS_Q + (gid * 512 + (lane >> 5) * 128 + 32 * t + (lane & 31)) * 16;
        const int ho = hval ? WS_H + ((t * 7 + (lane >> 2)) * 448 + own * 4 + (lane & 3)) * 16 : qo;
        v4u x0, x1;
        wait_prog(nW + QPOLL);
        if (4 * (t + 1) <= QPOLL) {
#pragma unroll
            for (int i = 0; i < 16; ++i) kf[i] = lds128(ka + (i >> 3) * 8448 + (i & 7) * 64);
        }
#pragma unroll 1
        for (unsigned it = 0; it < SPIN_CAP; ++it) {
            cbar();
            x0 = ldg(rs, qo); x1 = ldg(rs, qo + 256 * 16); hx = ldg(rs, ho);
            if (__all(tags_ok(x0, T0, T1) & tags_ok(x1, T0, T1) & (!hval | tags_ok(hx, T0, T1)))) break;
        }
        const float q0 = x32_sum(bflo(x0.x) + bflo(x1.x)), q1 = x32_sum(bfhi(x0.x) + bfhi(x1.x));
        const float q2 = x32_sum(bflo(x0.z) + bflo(x1.z)), q3 = x32_sum(bfhi(x0.z) + bfhi(x1.z));
        if (lane < 32) *(LAS v2u*)(L_Q + (32 * t + lane) * 8) = v2u{cvtpk(q0, q1), cvtpk(q2, q3)};
        lds_signal(F_Q);
    }
    {
        if (4 * (t + 1) > QPOLL) {
            wait_prog(nW + 4 * (t + 1));
#pragma unroll
            for (int i = 0; i < 16; ++i) kf[i] = lds128(ka + (i >> 3) * 8448 + (i & 7) * 64);
        }
        spin_lds_ge(F_Q, 4);
        v4u qf[8];
#pragma unroll
        for (int ks = 0; ks < 8; ++ks) qf[ks] = lds128(L_Q + (n & 1) * 512 + ks * 64 + g * 16);
        __builtin_amdgcn_sched_barrier(0);
        f32x4 z0 = {0.f, 0.f, 0.f, 0.f}, z1 = {0.f, 0.f, 0.f, 0.f};
#pragma unroll
        for (int ks = 0; ks < 8; ++ks) { z0 = mfma(kf[ks], qf[ks], z0); z1 = mfma(kf[8 + ks], qf[ks], z1); }
        constexpr float C = 0.0625f * 1.4426950408889634f;
        const float z[4] = {(n & 2) ? z1[0] : z0[0], (n & 2) ? z1[1] : z0[1], (n & 2) ? z1[2] : z0[2], (n & 2) ? z1[3] : z0[3]};
        float m = fmx(fmx(z[0], z[1]), fmx(z[2], z[3])) * C;
        m = xrow_max(fmx(m, dpp<XOR2>(m)));
        float p[4];
#pragma unroll
        for (int r = 0; r < 4; ++r) p[r] = __builtin_amdgcn_exp2f(fmaf(z[r], C, -m));
        if (n < 4) *(LAS v2u*)(L_P + (n & 1) * 256 + (32 * t + 8 * g + 4 * (n >> 1)) * 2) = v2u{cvtpk(p[0], p[1]), cvtpk(p[2], p[3])};
        float l = (p[0] + p[1]) + (p[2] + p[3]);
        l = xrow_sum(l + dpp<XOR2>(l));
        if (lane < 2) { *(LAS float*)(L_MX + lane * 16 + t * 4) = m; *(LAS float*)(L_LS + lane * 16 + t * 4) = l; }
        lds_signal(F_P);
    }
    if (mlpo) {
        const float hv[4] = {hval ? bflo(hx.x) : 0.f, hval ? bfhi(hx.x) : 0.f, hval ? bflo(hx.z) : 0.f, hval ? bfhi(hx.z) : 0.f};
        const float bb[4] = {bflo(b1v.x), bfhi(b1v.x), bflo(b1v.y), bfhi(b1v.y)};
        float act[4];
#pragma unroll
        for (int r = 0; r < 4; ++r) {
            float v = hv[r]; v += dpp<ROR8>(v); v += dpp<ROR4>(v); v = x16_sum(v) + bb[r];
            const float e = __builtin_amdgcn_exp2f(-1.4426950408889634f * fabsf(v));
            const float num = v > 0.f ? fmaf(2.f, e, 1.f) : e * (e + 2.f);
            const float den = v > 0.f ? fmaf(2.f * e, e, num) : num + 2.f;
            act[r] = v * num * __builtin_amdgcn_rcpf(den);
        }
        if (lane < 4) *(LAS v2u*)(L_AM + t * 32 + lane * 8) = v2u{cvtpk(act[0], act[1]), cvtpk(act[2], act[3])};
        lds_signal(F_AM);
    }
    {
        const unsigned e = r1 & 1;
        const unsigned vb = L_V + (2 * g + (r1 >> 1)) * 1088 + e * 512 + t * 128 + c4 * 8;
        const unsigned va0 = vb + e * 32, va1 = vb + (e ^ 1) * 32;
        const int ooff = WS_O + ((((kvh * 16 + (n & 1) * 8 + 2 * t + ((n >> 2) & 1)) * 4 + b) * 8 + s) * 8 + 4 * ((n >> 1) & 1) + g) * 16;
        const int soff = WS_S + ((((kvh * 2 + (lane & 1)) * 4 + b) * 8 + s) * 4 + ((lane >> 1) & 3)) * 16;
        int ooffp = ooff, soffp = soff;
        asm volatile("" : "+v"(ooffp), "+v"(soffp));
        spin_lds_ge(F_P, 4);
        v4u pf[4];
#pragma unroll
        for (int kk = 0; kk < 4; ++kk) pf[kk] = lds128(L_P + (n & 1) * 256 + kk * 64 + g * 16);
        const v4u mx = lds128(L_MX + (n & 1) * 16), ls = lds128(L_LS + (n & 1) * 16);
        const float mk[4] = {__uint_as_float(mx.x), __uint_as_float(mx.y), __uint_as_float(mx.z), __uint_as_float(mx.w)};
        const float mW = fmx(fmx(mk[0], mk[1]), fmx(mk[2], mk[3]));
        float fk[4];
#pragma unroll
        for (int kk = 0; kk < 4; ++kk) fk[kk] = __builtin_amdgcn_exp2f(mk[kk] - mW);
        const float lW = fmaf(fk[0], __uint_as_float(ls.x), fk[1] * __uint_as_float(ls.y)) + fmaf(fk[2], __uint_as_float(ls.z), fk[3] * __uint_as_float(ls.w));
        if (t == 0 && lane < 8) stg(rs, soffp, __float_as_uint(mW), __float_as_uint(lW), T0, T1);
        f32x4 o[4];
        unsigned seen = 0;
        v4u vf[2][4];
        wait_prog_c(nW + 20, seen);
#pragma unroll
        for (int aa = 0; aa < 4; ++aa) { const unsigned ad = ((aa & 1) ? va1 : va0) + (aa >> 1) * 64; vf[0][aa] = trfrag(ad, ad + 8704); }
#pragma unroll
        for (int kk = 0; kk < 4; ++kk) {
            if (kk < 3) {
                wait_prog_c(nW + 16 + 4 * (kk + 2), seen);
#pragma unroll
                for (int aa = 0; aa < 4; ++aa) { const unsigned ad = ((aa & 1) ? va1 : va0) + (aa >> 1) * 64 + (kk + 1) * 17408; vf[(kk + 1) & 1][aa] = trfrag(ad, ad + 8704); }
            }
#pragma unroll
            for (int aa = 0; aa < 4; ++aa) { const f32x4 d = mfma(vf[kk & 1][aa], pf[kk], f32x4{0.f, 0.f, 0.f, 0.f}); o[aa] = kk == 0 ? fk[0] * d : o[aa] + fk[kk] * d; }
            asm volatile("" : "+v"(o[0]), "+v"(o[1]), "+v"(o[2]), "+v"(o[3]));
        }
        float v[4];
#pragma unroll
        for (int r = 0; r < 4; ++r) { const float lo = (n & 2) ? o[1][r] : o[0][r], hi = (n & 2) ? o[3][r] : o[2][r]; v[r] = (n & 4) ? hi : lo; }
        if (n < 8) stg(rs, ooffp, cvtpk(v[0], v[1]), cvtpk(v[2], v[3]), T0, T1);
    }
    if (owner) {
        const unsigned ba = L_K + (4 * g + r1) * 928 + t * 224 + c4 * 8;
        const int q4 = n >> 2, h = j >> 3;
        const int oo = WS_O + ((own * 4 + t) * 64 + lane) * 16, so = WS_S + (((kvh * 2 + h) * 4 + t) * 8 + (lane >> 3)) * 64;
        const int foff0 = WS_FIN + ((((n & 3) * 28 + 7 * t + q4) * 128 + own) * 4 + g) * 16;
        unsigned ab[4];
#pragma unroll
        for (int q = 0; q < 4; ++q) ab[q] = q4 == q ? L_A + (n & 3) * 64 + g * 16 : L_Z;
        const v4u sel = v4u{n == 0 ? 0x00003F80u : n == 1 ? 0x3F800000u : 0u, 0u, n == 2 ? 0x00003F80u : n == 3 ? 0x3F800000u : 0u, 0u};
        v4u selp = sel;
        int foffp = foff0;
        unsigned aw = L_A + t * 64 + (8 * (lane & 3) + 4 * (lane >> 2)) * 2;
        asm volatile("" : "+v"(selp), "+v"(foffp), "+v"(aw), "+v"(ab[0]), "+v"(ab[1]), "+v"(ab[2]), "+v"(ab[3]));
        f32x4 acc0 = {0.f, 0.f, 0.f, 0.f}, acc1 = {0.f, 0.f, 0.f, 0.f};
        v4u wf[7], og, sg;
        float wgt = 0.f;
        wait_prog(nW + 32 + 4);
        if (mlpo) {
            wait_lds_ge(F_AM, 4);
            const v2u am = lds64(L_AM + (n & 3) * 32 + g * 8);
#pragma unroll
            for (int T = 0; T < 7; ++T) {
                const v2u wl = tr64(ba + T * 32);
                const bool on = q4 == (T & 3);
                const f32x4 d = mfma(v4u{wl.x, wl.y, 0u, 0u}, v4u{on ? am.x : 0u, on ? am.y : 0u, 0u, 0u}, T < 4 ? acc0 : acc1);
                if (T < 4) acc0 = d; else acc1 = d;
            }
        }
        wait_prog(nW + 32 + 12);
#pragma unroll
        for (int T = 0; T < 7; ++T) wf[T] = trfrag(ba + 16 * 928 + T * 32, ba + 32 * 928 + T * 32);
        bool havew = false;
        f32x4 ds;
#pragma unroll 1
        for (unsigned it = 0; it < SPIN_CAP; ++it) {
            cbar();
            og = ldg(rs, oo);
            if (!havew) {
                sg = ldg(rs, so);
                if (__all(tags_ok(sg, T0, T1))) {
                    const float m = __uint_as_float(sg.x), l = __uint_as_float(sg.z);
                    const float M = xrow_max(fmx(m, dpp<ROR8>(m))), e = __builtin_amdgcn_exp2f(m - M);
                    float le = l * e; le += dpp<ROR8>(le);
                    wgt = e * __builtin_amdgcn_rcpf(xrow_sum(le));
                    havew = true;
                }
            }
            const v4u pb = v4u{cvtpk(bflo(og.x) * wgt, bfhi(og.x) * wgt), 0u, cvtpk(bflo(og.z) * wgt, bfhi(og.z) * wgt), 0u};
            ds = mfma(selp, pb, f32x4{0.f, 0.f, 0.f, 0.f});
            if (havew && __all(tags_ok(og, T0, T1))) break;
        }
        float v[4];
#pragma unroll
        for (int r = 0; r < 4; ++r) v[r] = ds[r] + dpp<ROR8>(ds[r]);
        if (lane < 8) *(LAS v2u*)aw = v2u{cvtpk(v[0], v[1]), cvtpk(v[2], v[3])};
        lds_signal(F_A);
        spin_lds_ge(F_A, 4);
        v4u B[4];
#pragma unroll
        for (int q = 0; q < 4; ++q) B[q] = lds128(ab[q]);
        __builtin_amdgcn_sched_barrier(0);
#pragma unroll
        for (int T = 0; T < 4; ++T) { acc0 = mfma(wf[T], B[T], acc0); if (T < 3) acc1 = mfma(wf[4 + T], B[T], acc1); }
        stg(rs, foffp, cvtpk(acc0[0], acc0[1]), cvtpk(acc0[2], acc0[3]), T0, T1);
        if (q4 < 3) stg(rs, foffp + 4 * 128 * 4 * 16, cvtpk(acc1[0], acc1[1]), cvtpk(acc1[2], acc1[3]), T0, T1);
    }
    if (t == 3 && fin) {
        const int fo = WS_FIN + ((fb * 28 + fc) * 512 + lane) * 16;
        v4u sel = v4u{n == 0 ? 0x00003F80u : n == 1 ? 0x3F800000u : 0u, 0u, n == 2 ? 0x00003F80u : n == 3 ? 0x3F800000u : 0u, 0u};
        float xr[4] = {bflo(xres.x), bfhi(xres.x), bflo(xres.y), bfhi(xres.y)};
        unsigned long long op = (unsigned long long)(a.out + fb * 448 + 16 * fc + 4 * (lane & 3));
        asm volatile("" : "+v"(sel), "+v"(xr[0]), "+v"(xr[1]), "+v"(xr[2]), "+v"(xr[3]), "+v"(op));
        {
#pragma unroll 1
            for (unsigned it = 0; it < SPIN_CAP; ++it) {
                cbar();
                bool ok = true;
                if (lane < 8) ok = tags_ok(ldg(rs, WS_FIN + ((fb * 28 + fc) * 512 + (lane * 16 + 15) * 4) * 16), T0, T1);
                if (__all(ok)) break;
                __builtin_amdgcn_s_sleep(FPROBE);
            }
        }
        f32x4 d0, d1;
#pragma unroll 1
        for (unsigned it = 0; it < SPIN_CAP; ++it) {
            cbar();
            v4u f[8];
            bool ok = true;
#pragma unroll
            for (int i = 0; i < 8; ++i) f[i] = ldg(rs, fo + i * 1024);
            d0 = f32x4{0.f, 0.f, 0.f, 0.f}; d1 = f32x4{0.f, 0.f, 0.f, 0.f};
#pragma unroll
            for (int i = 0; i < 8; i += 2) {
                ok &= tags_ok(f[i], T0, T1) & tags_ok(f[i + 1], T0, T1);
                d0 = mfma(sel, f[i], d0); d1 = mfma(sel, f[i + 1], d1);
            }
            if (__all(ok)) break;
        }
        float v[4];
#pragma unroll
        for (int r = 0; r < 4; ++r) { float x = d0[r] + d1[r]; x += dpp<ROR8>(x); v[r] = x + dpp<ROR4>(x); }
        if (lane < 4) {
            *(GAS v2u*)op = v2u{cvtpk(xr[0] + v[0], xr[1] + v[1]), cvtpk(xr[2] + v[2], xr[3] + v[3])};
        }
        if (blk == 128 && lane == 0) __hip_atomic_store((GAS unsigned*)(a.ws + WS_E), Eu + 1u, __ATOMIC_RELAXED, __HIP_MEMORY_SCOPE_AGENT);
    }
}
}

extern "C" void kernel_launch(void* const* d_in, const int* in_sizes, int n_in, void* d_out, int out_size, void* d_ws, size_t ws_size, hipStream_t stream) {
    (void)in_sizes; (void)n_in; (void)out_size; (void)ws_size;
    fk::Args a;
    a.x = (const unsigned char*)d_in[0]; a.k = (const unsigned char*)d_in[1]; a.v = (const unsigned char*)d_in[2];
    a.wq = (const unsigned char*)d_in[3]; a.wo = (const unsigned char*)d_in[4]; a.w1 = (const unsigned char*)d_in[6];
    a.b1 = (const unsigned char*)d_in[7]; a.w2 = (const unsigned char*)d_in[8]; a.out = (unsigned short*)d_out; a.ws = (unsigned char*)d_ws;
    (void)hipFuncSetAttribute((const void*)fk::fused, hipFuncAttributeMaxDynamicSharedMemorySize, fk::LDS_BYTES);
    hipLaunchKernelGGL(fk::fused, dim3(256), dim3(512), fk::LDS_BYTES, stream, a);
}
```
